```python
import torch

def _softmax(x, axis=-1):
    return torch.softmax(x, dim=axis)

def _gelu(x):
    return torch.nn.functional.gelu(x, approximate='tanh')

def _silu(x):
    return torch.nn.functional.silu(x)

def _sigmoid(x):
    return torch.sigmoid(x)

def _layernorm(x, axis=-1, eps=1e-05):
    m = x.mean(dim=axis, keepdim=True)
    v = x.var(dim=axis, keepdim=True, unbiased=False)
    return (x - m) / torch.sqrt(v + eps)

def _scatter_add(tgt, idx, src, axis=0):
    return tgt.clone().index_add_(axis, idx, src)

def _segment_sum(src, seg, n):
    out = torch.zeros((n,) + src.shape[1:], dtype=src.dtype)
    return out.index_add_(0, seg, src)

def _pad(x, pw):
    flat = tuple((v for pair in reversed(pw) for v in pair))
    return torch.nn.functional.pad(x, flat)
kernel_name = 'parallel_sum__memory_aug_recurrent_mem_tr__mixer_channel_mlp__prompt'

def setup_inputs():

    _w = lambda a: (lambda t: t.to(torch.bfloat16) if t.is_floating_point() else t)(torch.as_tensor(a))

    def _A_setup():
        B, S, Dm = (1, 2048, 512)
        H, Dh = (32, 128)
        M, seg = (8, 32)
        seg = min(seg, S)
        S = S // seg * seg
        return {'x': _w((lambda *s: torch.randn(*s, dtype=torch.bfloat16) * 0.05)(B, S, Dm)), 'mem0': _w((lambda *s: torch.randn(*s, dtype=torch.bfloat16) * 0.2)(B, M, Dm)), 'Wq': _w((lambda *s: torch.randn(*s, dtype=torch.bfloat16) * 1.6)(Dm, H, Dh) * Dm ** (-0.5)), 'Wk': _w((lambda *s: torch.randn(*s, dtype=torch.bfloat16) * 1.6)(Dm, H, Dh) * Dm ** (-0.5)), 'Wv': _w((lambda *s: torch.randn(*s, dtype=torch.bfloat16) * 0.2)(Dm, H, Dh) * Dm ** (-0.5)), 'Wo': _w((lambda *s: torch.randn(*s, dtype=torch.bfloat16) * 0.2)(H, Dh, Dm) * (H * Dh) ** (-0.5)), 'seg_len': seg}
    _A_in = _A_setup()

    def _B_setup():
        B, S, D, F = (1, 2048, 512, 1792)
        return {'x': _w((lambda *s: torch.randn(*s, dtype=torch.bfloat16) * 0.05)(B, S, D)), 'w1': _w((lambda *s: torch.randn(*s, dtype=torch.bfloat16) * 0.8)(D, F) * D ** (-0.5)), 'b1': _w((lambda *s: torch.randn(*s, dtype=torch.bfloat16) * 0.4)(F)), 'w2': _w((lambda *s: torch.randn(*s, dtype=torch.bfloat16) * 0.05)(F, D) * F ** (-0.5)), 'b2': _w((lambda *s: torch.randn(*s, dtype=torch.bfloat16) * 0.05)(D))}
    _B_in = _B_setup()
    return {'A_x': _A_in['x'], 'A_mem0': _A_in['mem0'], 'A_Wq': _A_in['Wq'], 'A_Wk': _A_in['Wk'], 'A_Wv': _A_in['Wv'], 'A_Wo': _A_in['Wo'], 'A_seg_len': _A_in['seg_len'], 'B_x': _B_in['x'], 'B_w1': _B_in['w1'], 'B_b1': _B_in['b1'], 'B_w2': _B_in['w2'], 'B_b2': _B_in['b2']}

def reference(A_x, A_mem0, A_Wq, A_Wk, A_Wv, A_Wo, A_seg_len, B_x, B_w1, B_b1, B_w2, B_b2):

    def _A_ref(x, mem0, Wq, Wk, Wv, Wo, seg_len):
        B, S, Dm = x.shape
        H, Dh = (Wq.shape[1], Wq.shape[2])
        M = mem0.shape[1]
        scale = Dh ** (-0.5)
        n_seg = S // seg_len
        L = M + seg_len
        idx = torch.arange(L)
        mask = (idx[:, None] < M) | (idx[:, None] >= idx[None, :])
        mem = mem0
        outs = []
        for s in range(n_seg):
            xs = x[:, s * seg_len:(s + 1) * seg_len, :].clone()
            h = torch.concatenate([mem, xs], axis=1)
            q = torch.einsum('btd,dhe->bhte', h, Wq)
            k = torch.einsum('btd,dhe->bhte', h, Wk)
            v = torch.einsum('btd,dhe->bhte', h, Wv)
            scores = torch.einsum('bhqe,bhke->bhqk', q, k) * scale
            scores = torch.where(mask[None, None], scores, -1000000000.0)
            p = _softmax(scores, axis=-1)
            o = torch.einsum('bhqk,bhke->bhqe', p, v)
            o = torch.einsum('bhte,hed->btd', o, Wo)
            mem = o[:, :M, :].clone()
            outs.append(o[:, M:, :])
        return torch.concatenate(outs, axis=1)

    def _B_ref(x, w1, b1, w2, b2):
        y = _layernorm(x, axis=-1)
        h = _gelu(torch.einsum('bsd,df->bsf', y, w1) + b1)
        return torch.einsum('bsf,fd->bsd', h, w2) + b2
    x = A_x
    xn = _layernorm(x, axis=-1)
    return x + _A_ref(xn, A_mem0, A_Wq, A_Wk, A_Wv, A_Wo, A_seg_len) + _B_ref(xn, B_w1, B_b1, B_w2, B_b2)
```

```cpp
#include <hip/hip_runtime.h>
#include <hip/hip_bf16.h>
#include <stdint.h>

#define GAS __attribute__((address_space(1)))
#define LAS __attribute__((address_space(3)))
#define DEV __device__ __forceinline__
typedef unsigned v4u __attribute__((ext_vector_type(4)));
typedef unsigned v2u __attribute__((ext_vector_type(2)));
typedef unsigned v8u __attribute__((ext_vector_type(8)));
typedef float f32x4 __attribute__((ext_vector_type(4)));
typedef __bf16 bf16x2 __attribute__((ext_vector_type(2)));
typedef __bf16 bf16x8 __attribute__((ext_vector_type(8)));
typedef __bf16 bf16x16 __attribute__((ext_vector_type(16)));
typedef short s16x4 __attribute__((ext_vector_type(4)));
typedef LAS unsigned char* lptr;

namespace {

constexpr int NSEG = 64;
constexpr int MB = 1 << 20;
constexpr int OFF_G = 0;
constexpr int OFF_R = 2 * MB;
constexpr int OFF_FLAG = 4 * MB;
constexpr int OFF_XN = 5 * MB;
constexpr int OFF_Y = 7 * MB;
constexpr int OFF_Q = 16 * MB;
constexpr int OFF_K = 32 * MB;
constexpr int OFF_V = 48 * MB;
constexpr int OFF_KM = 64 * MB;
constexpr int OFF_VM = 72 * MB;
constexpr int OFF_PART = 80 * MB;
constexpr int NPART = 46;
constexpr int WS_END = OFF_PART + NSEG * NPART * 32768;
constexpr int FL_ABORT = 0, FL_QKV = 8, FL_MLP = 104, FL_KM = 136, FL_F = 200, FL_END = 240;
constexpr int OFF_PAR = OFF_FLAG + 31744;
constexpr int OFF_DBG = OFF_FLAG + 32768;
constexpr unsigned MARK = 0xFFFFFFFFu;
constexpr unsigned SPIN_CAP = 1u << 17;
constexpr int AUX_SC1 = 16;
constexpr int LDS_BYTES = 160 * 1024;

struct Params {
  const unsigned short *x, *mem0, *wq, *wk, *wv, *wo, *w1, *b1, *w2, *b2;
  unsigned short* out;
  unsigned char* ws;
};

typedef __amdgpu_buffer_rsrc_t rsrc_t;
DEV rsrc_t make_rsrc(const void* p, int bytes) { return __builtin_amdgcn_make_buffer_rsrc((void*)p, 0, bytes, 0x00020000); }
DEV v4u ld16(rsrc_t r, int off) { return __builtin_amdgcn_raw_buffer_load_b128(r, off, 0, AUX_SC1); }
DEV v2u ld8(rsrc_t r, int off) { return __builtin_amdgcn_raw_buffer_load_b64(r, off, 0, AUX_SC1); }
DEV unsigned ld4(rsrc_t r, int off) { return __builtin_amdgcn_raw_buffer_load_b32(r, off, 0, AUX_SC1); }
DEV void st16(v4u v, rsrc_t r, int off) { __builtin_amdgcn_raw_buffer_store_b128(v, r, off, 0, AUX_SC1); }
DEV void st8(v2u v, rsrc_t r, int off) { __builtin_amdgcn_raw_buffer_store_b64(v, r, off, 0, AUX_SC1); }
DEV void st4(unsigned v, rsrc_t r, int off) { __builtin_amdgcn_raw_buffer_store_b32(v, r, off, 0, AUX_SC1); }

DEV float lo16(unsigned u) { return __builtin_bit_cast(float, u << 16); }
DEV float hi16(unsigned u) { return __builtin_bit_cast(float, u & 0xffff0000u); }
DEV unsigned pk2(float a, float b) { bf16x2 v = {(__bf16)a, (__bf16)b}; return __builtin_bit_cast(unsigned, v); }
DEV float rbf(float f) { return (float)(__bf16)f; }
DEV unsigned nomark(unsigned u) { return u < 0xFFFFFFFEu ? u : 0xFFFFFFFEu; }

DEV f32x4 mfma32(v4u a, v4u b, f32x4 c) {
  return __builtin_amdgcn_mfma_f32_16x16x32_bf16(__builtin_bit_cast(bf16x8, a), __builtin_bit_cast(bf16x8, b), c, 0, 0, 0);
}
DEV f32x4 mfma16(v2u a, v2u b, f32x4 c) {
  return __builtin_amdgcn_mfma_f32_16x16x16bf16_1k(__builtin_bit_cast(s16x4, a), __builtin_bit_cast(s16x4, b), c, 0, 0, 0);
}
DEV f32x4 smf(v4u a, v8u b, f32x4 c, int idx) {
  return __builtin_amdgcn_smfmac_f32_16x16x64_bf16(__builtin_bit_cast(bf16x8, a), __builtin_bit_cast(bf16x16, b), c, idx, 0, 0);
}
#define BAR() asm volatile("s_waitcnt lgkmcnt(0)\n\ts_barrier" ::: "memory")
#define WAIT_VM(n) asm volatile("s_waitcnt vmcnt(%0)" ::"n"(n) : "memory")
DEV v4u lds16(lptr p) { return *(LAS v4u*)p; }
DEV void lds16w(lptr p, v4u v) { *(LAS v4u*)p = v; }
DEV v2u trread(lptr p) {
  return __builtin_bit_cast(v2u, __builtin_amdgcn_ds_read_tr16_b64_v4i16((LAS s16x4*)p));
}
DEV float xrow8(float v) { return __builtin_bit_cast(float, __builtin_amdgcn_update_dpp(0, __builtin_bit_cast(int, v), 0x128, 0xf, 0xf, true)); }
DEV unsigned uq1(unsigned v) { return (unsigned)__builtin_amdgcn_update_dpp(0, (int)v, 0xB1, 0xf, 0xf, true); }
DEV float asf(unsigned u) { return __builtin_bit_cast(float, u); }
DEV unsigned asu(float f) { return __builtin_bit_cast(unsigned, f); }
DEV float max16(float v) { auto r = __builtin_amdgcn_permlane16_swap(asu(v), asu(v), false, false); const unsigned a = r[0], b = r[1]; return fmaxf(asf(a), asf(b)); }
DEV float max32(float v) { auto r = __builtin_amdgcn_permlane32_swap(asu(v), asu(v), false, false); const unsigned a = r[0], b = r[1]; return fmaxf(asf(a), asf(b)); }
DEV float sum16(float v) { auto r = __builtin_amdgcn_permlane16_swap(asu(v), asu(v), false, false); const unsigned a = r[0], b = r[1]; return asf(a) + asf(b); }
DEV float sum32(float v) { auto r = __builtin_amdgcn_permlane32_swap(asu(v), asu(v), false, false); const unsigned a = r[0], b = r[1]; return asf(a) + asf(b); }
DEV float pairsum(float x, float y) { auto r = __builtin_amdgcn_permlane32_swap(asu(x), asu(y), false, false); const unsigned a = r[0], b = r[1]; return asf(a) + asf(b); }

template <int I, int N, class F> DEV void sfor(F&& f) { if constexpr (I < N) { f(std::integral_constant<int, I>{}); sfor<I + 1, N>(f); } }

DEV void glds(const void* base, unsigned voff, unsigned dst) {
  unsigned keep;
  asm volatile("s_mov_b32 %0, m0\n\ts_mov_b32 m0, %3\n\ts_nop 0\n\tglobal_load_lds_dwordx4 %1, %2\n\ts_mov_b32 m0, %0"
               : "=&s"(keep) : "v"(voff), "s"(base), "s"(dst) : "memory");
}
DEV unsigned swz(unsigned row) { return ((row & 3) << 2) | ((row >> 2) & 3); }
DEV void stage_chunk(const void* base, unsigned stride, unsigned slot, int w, int lane) {
#pragma unroll
  for (int q = 0; q < 4; ++q) {
    const unsigned ii = 4 * w + q, row = 4 * ii + (lane >> 4);
    glds(base, row * stride + (((lane & 15) ^ swz(row)) << 4), slot + ii * 1024);
  }
}
DEV v2u trfrag(lptr slot, unsigned row, unsigned c4) { return trread(slot + 256 * row + 16 * ((c4 >> 1) ^ swz(row)) + 8 * (c4 & 1)); }

DEV bool spin_fail(unsigned& spins, rsrc_t rs, unsigned code) {
  ++spins;
  if (spins > SPIN_CAP) { st4(code, rs, OFF_FLAG + FL_ABORT * 128); return true; }
  if ((spins & 255u) == 0u) return __builtin_amdgcn_readfirstlane(ld4(rs, OFF_FLAG + FL_ABORT * 128)) != 0u;
  return false;
}
DEV void wait_flags(rsrc_t rs, int foff, unsigned need, unsigned code) {
  unsigned spins = 0;
  for (;;) {
    asm volatile("" ::: "memory");
    const unsigned v = ld4(rs, foff);
    if (__all(v >= need)) break;
    if (spin_fail(spins, rs, code)) break;
    __builtin_amdgcn_s_sleep(1);
  }
}

DEV float wg_rowsum(float v, LAS float* red, int w, int lane) {
  v = sum32(sum16(v));
  __syncthreads();
  if (lane < 16) red[w * 16 + lane] = v;
  __syncthreads();
  const int r = lane & 15;
  return (red[r] + red[16 + r]) + (red[32 + r] + red[48 + r]);
}
DEV void layernorm32(float (&f)[32], LAS float* red, int w, int lane) {
  float s = 0.f;
#pragma unroll
  for (int i = 0; i < 32; ++i) s += f[i];
  const float mean = wg_rowsum(s, red, w, lane) * (1.f / 512.f);
  float q = 0.f;
#pragma unroll
  for (int i = 0; i < 32; ++i) { const float d = f[i] - mean; q += d * d; }
  const float var = wg_rowsum(q, red, w, lane) * (1.f / 512.f);
  const float mb = rbf(mean), den = rbf(sqrtf(rbf(rbf(var) + 1e-5f)));
  const float inv = 1.f / den;
#pragma unroll
  for (int i = 0; i < 32; ++i) f[i] = rbf(rbf(f[i] - mb) * inv);
}

__global__ void __launch_bounds__(256) k_prep(Params p) {
  __shared__ float red[64];
  const int b = blockIdx.x, tid = threadIdx.x, lane = tid & 63, w = tid >> 6;
  GAS unsigned char* ws = (GAS unsigned char*)p.ws;
  if (b < 128) {
    const int r = lane & 15, g = lane >> 4;
    float f[32];
#pragma unroll
    for (int i = 0; i < 4; ++i) {
      const v4u v = *(const GAS v4u*)((const GAS unsigned short*)p.x + (size_t)(16 * b + r) * 512 + 32 * (4 * w + i) + 8 * g);
#pragma unroll
      for (int j = 0; j < 4; ++j) { f[8 * i + 2 * j] = lo16(v[j]); f[8 * i + 2 * j + 1] = hi16(v[j]); }
    }
#pragma unroll
    for (int pass = 0; pass < 2; ++pass) {
      layernorm32(f, (LAS float*)red, w, lane);
#pragma unroll
      for (int i = 0; i < 4; ++i) {
        v4u o;
#pragma unroll
        for (int j = 0; j < 4; ++j) o[j] = pk2(f[8 * i + 2 * j], f[8 * i + 2 * j + 1]);
        *(GAS v4u*)(ws + (pass ? OFF_Y : OFF_XN) + (b * 16 + 4 * w + i) * 1024 + lane * 16) = o;
      }
    }
    return;
  }
  const int c = b - 128;
  const v4u mk = {MARK, MARK, MARK, MARK};
  for (int i = c * 256 + tid; i < (65 * 16384) / 16; i += 128 * 256) *(GAS v4u*)(ws + OFF_G + i * 16) = mk;
  for (int i = c * 256 + tid; i < MB / 16; i += 128 * 256) *(GAS v4u*)(ws + OFF_R + i * 16) = mk;
  if (c == 1) for (int i = tid; i < FL_END * 128 / 16; i += 256) *(GAS v4u*)(ws + OFF_FLAG + i * 16) = v4u{0u, 0u, 0u, 0u};
  if (c == 0 && w == 0) {
    const rsrc_t rs = make_rsrc(p.ws, WS_END);
#pragma unroll 1
    for (int page = 0; page < 2; ++page) {
      unsigned best[2] = {~0u, ~0u};
#pragma unroll
      for (int par = 0; par < 2; ++par)
#pragma unroll 1
        for (int k = 0; k < 3; ++k) {
          const int off = page * 2 * MB + (96 + k) * 16384 + par * 8192;
          st4(1u, rs, off);
          WAIT_VM(0);
          const unsigned long long t0 = __builtin_amdgcn_s_memtime();
          unsigned v = ld4(rs, off);
          asm volatile("s_waitcnt vmcnt(0)" : "+v"(v)::"memory");
          const unsigned dt = (unsigned)(__builtin_amdgcn_s_memtime() - t0);
          best[par] = dt < best[par] ? dt : best[par];
        }
      if (lane == 0) *(GAS unsigned*)(ws + OFF_PAR + page * 4) = best[1] < best[0] ? 1u : 0u;
    }
  }
}

constexpr int CH_GX = 128 * 1024, CH_SC = CH_GX + 8192, CH_OA = CH_SC + 12288;

DEV v8u pin_acc(v8u x) { v8u y; asm volatile("" : "=a"(y) : "0"(x)); return y; }
#define SMF_ACC(acc, a, b, idx) asm volatile("v_smfmac_f32_16x16x64_bf16 %0, %1, %2, %3" : "+v"(acc) : "v"(a), "a"(b), "v"(idx))
#define SMF_VEC(acc, a, b, idx) asm volatile("v_smfmac_f32_16x16x64_bf16 %0, %1, %2, %3" : "+v"(acc) : "v"(a), "v"(b), "v"(idx))

DEV bool has_mark(v4u a) { return max(max(a.x, a.y), max(a.z, a.w)) == MARK; }

DEV void role_chain(const Params& p, int c, lptr L) {
  const int tid = threadIdx.x, lane = tid & 63, w = __builtin_amdgcn_readfirstlane(tid >> 6);
  const int h = c >> 1, ch = c & 1;
  const int n = lane & 15, g = lane >> 4;
  const rsrc_t rs = make_rsrc(p.ws, WS_END);
  const unsigned Lb = (unsigned)(size_t)L;
  const int par0 = *(const GAS int*)((GAS unsigned char*)p.ws + OFF_PAR), par1 = *(const GAS int*)((GAS unsigned char*)p.ws + OFF_PAR + 4);

  v8u wq[2][8], wk[2][8], wo[4][2];
  {
    const int q = (lane >> 2) & 3, pp = lane & 3;
    auto src = [&](int ci, const void*& base, unsigned& stride) {
      if (ci < 8) { base = p.wq + (size_t)(64 * ci) * 4096 + h * 128; stride = 8192; }
      else if (ci < 16) { base = p.wk + (size_t)(64 * (ci - 8)) * 4096 + h * 128; stride = 8192; }
      else if (ci < 20) { base = p.wo + (size_t)(h * 128 + 64 * ((ci - 16) >> 1)) * 512 + 256 * ch + 128 * ((ci - 16) & 1); stride = 1024; }
      else { base = p.wv + (size_t)(64 * (ci - 20)) * 4096 + h * 128; stride = 8192; }
    };
    sfor<0, 8>([&](auto I) { const void* base; unsigned stride; src(I, base, stride); stage_chunk(base, stride, Lb + I * 16384, w, lane); });
    sfor<0, 28>([&](auto I) {
      constexpr int ci = I;
      constexpr int younger = (ci + 7 < 27 ? ci + 7 : 27) - ci;
      WAIT_VM(4 * younger);
      BAR();
      const lptr slot = L + (ci & 7) * 16384;
      auto row_d = [&](int qd) { return 16 * (2 * (qd >> 1) + (g >> 1)) + 8 * (qd & 1) + 4 * (q & 1) + 2 * (q >> 1) + (g & 1); };
      auto row_e = [&](int qd) { return 16 * (2 * (qd >> 1) + (g >> 1)) + 8 * (q >> 1) + 2 * (2 * (g & 1) + (qd & 1)) + (q & 1); };
      if constexpr (ci < 16) {
#pragma unroll
        for (int t = 0; t < 2; ++t) {
          v8u f;
#pragma unroll
          for (int qd = 0; qd < 4; ++qd) { const v2u v = trfrag(slot, row_d(qd), 8 * w + 2 * pp + t); f[2 * qd] = v.x; f[2 * qd + 1] = v.y; }
          if constexpr (ci < 8) wq[t][ci] = pin_acc(f); else wk[t][ci - 8] = pin_acc(f);
        }
      } else if constexpr (ci < 20) {
        constexpr int kk = (ci - 16) >> 1, colhalf = (ci - 16) & 1;
        if ((w >> 1) == colhalf) {
#pragma unroll
          for (int t = 0; t < 4; ++t) {
            v8u f;
#pragma unroll
            for (int qd = 0; qd < 4; ++qd) { const v2u v = trfrag(slot, row_e(qd), 16 * (w & 1) + 4 * pp + t); f[2 * qd] = v.x; f[2 * qd + 1] = v.y; }
            wo[t][kk] = f;
          }
        }
      } else {
        v8u f[2];
#pragma unroll
        for (int t = 0; t < 2; ++t)
#pragma unroll
          for (int qd = 0; qd < 4; ++qd) { const v2u v = trfrag(slot, row_d(qd), 8 * w + 4 * t + pp); f[t][2 * qd] = v.x; f[t][2 * qd + 1] = v.y; }
        asm volatile("" : "+v"(f[0]), "+v"(f[1]));
        BAR();
#pragma unroll
        for (int t = 0; t < 2; ++t) {
          lds16w(slot + (w * 2 + t) * 2048 + lane * 16, v4u{f[t][0], f[t][1], f[t][2], f[t][3]});
          lds16w(slot + (w * 2 + t) * 2048 + 1024 + lane * 16, v4u{f[t][4], f[t][5], f[t][6], f[t][7]});
        }
      }
      BAR();
      if constexpr (ci + 8 < 28) { const void* base; unsigned stride; src(ci + 8, base, stride); stage_chunk(base, stride, Lb + (ci & 7) * 16384, w, lane); }
    });
  }

  const int idx = (lane & 8) ? 0xEEEE : 0x4444;
  v2u sel0, sel1; v4u selr;
  {
    const unsigned one = 0x3F80u;
    auto pick = [&](int k0, int want) { return (k0 == want ? one : 0u) | (k0 + 1 == want ? one << 16 : 0u); };
    sel0 = v2u{pick(4 * g, n & 7), pick(4 * g + 2, n & 7)};
    sel1 = v2u{pick(4 * g, 8 + (n & 7)), pick(4 * g + 2, 8 + (n & 7))};
    selr = v4u{pick(0, n), pick(2, n), pick(4, n), pick(6, n)};
  }
  const int gbase = OFF_G + par0 * 8192;
  const int rbase = OFF_R + par1 * 8192;
  const int kvoff = h * 8192 + lane * 16;
  unsigned seenK = 0;
  auto kv_wait = [&](int s) {
    const unsigned need = (unsigned)(s >> 1) + 1;
    if (seenK < need) {
      const int foff = OFF_FLAG + (FL_QKV + (lane & 1) * 32 + h) * 128;
      unsigned spins = 0;
      for (;;) {
        asm volatile("" ::: "memory");
        const unsigned v = ld4(rs, foff);
        const unsigned a = __builtin_amdgcn_readlane(v, 0), b2 = __builtin_amdgcn_readlane(v, 1);
        seenK = a < b2 ? a : b2;
        if (seenK >= need) break;
        if (spin_fail(spins, rs, 0x100u + s)) break;
      }
    }
  };
  v4u kx[2], vx[2], kn[2], vn[2];
  kv_wait(0);
#pragma unroll
  for (int t = 0; t < 2; ++t) {
    kn[t] = ld16(rs, OFF_K + kvoff + (t * 4 + w) * 1024);
    vn[t] = ld16(rs, OFF_V + kvoff + (2 * w + t) * 1024);
  }

#pragma unroll 1
  for (int s = 0; s < NSEG; ++s) {
    v4u a0, a1;
    if (s == 0) {
      const GAS unsigned short* m0 = (const GAS unsigned short*)p.mem0 + (n & 7) * 512 + 16 * g + 2 * (n >> 3);
#pragma unroll
      for (int j = 0; j < 4; ++j) {
        const int e0 = 2 * j, e1 = 2 * j + 1;
        const int o0 = 4 * (e0 & 3) + (e0 >> 2), o1 = 4 * (e1 & 3) + (e1 >> 2);
        a0[j] = (unsigned)m0[64 * w + o0] | ((unsigned)m0[64 * w + o1] << 16);
        a1[j] = (unsigned)m0[64 * (4 + w) + o0] | ((unsigned)m0[64 * (4 + w) + o1] << 16);
      }
    } else {
      unsigned spins = 0;
      for (;;) {
        asm volatile("" ::: "memory");
        a0 = ld16(rs, gbase + s * 16384 + w * 1024 + lane * 16);
        a1 = ld16(rs, gbase + s * 16384 + (4 + w) * 1024 + lane * 16);
        if (!__any(has_mark(a0) || has_mark(a1))) break;
        if (spin_fail(spins, rs, 0x200u + s)) break;
      }
    }
    lds16w(L + CH_GX + w * 1024 + lane * 16, a0);
    lds16w(L + CH_GX + (4 + w) * 1024 + lane * 16, a1);
    BAR();
#pragma unroll
    for (int t = 0; t < 2; ++t) { kx[t] = kn[t]; vx[t] = vn[t]; }
    if (s + 1 < NSEG) {
      kv_wait(s + 1);
#pragma unroll
      for (int t = 0; t < 2; ++t) {
        kn[t] = ld16(rs, OFF_K + (s + 1) * 262144 + kvoff + (t * 4 + w) * 1024);
        vn[t] = ld16(rs, OFF_V + (s + 1) * 262144 + kvoff + (2 * w + t) * 1024);
      }
    }
    v4u A[8];
#pragma unroll
    for (int kk = 0; kk < 8; ++kk) A[kk] = lds16(L + CH_GX + kk * 1024 + lane * 16);
    f32x4 dq0 = {0.f, 0.f, 0.f, 0.f}, dq1 = dq0, dk0 = dq0, dk1 = dq0, dv0 = dq0, dv1 = dq0;
    asm volatile("s_nop 1" : "+v"(dq0), "+v"(dq1), "+v"(dk0), "+v"(dk1));
#pragma unroll
    for (int kk = 0; kk < 8; ++kk) {
      SMF_ACC(dq0, A[kk], wq[0][kk], idx);
      SMF_ACC(dk0, A[kk], wk[0][kk], idx);
      SMF_ACC(dq1, A[kk], wq[1][kk], idx);
      SMF_ACC(dk1, A[kk], wk[1][kk], idx);
    }
    asm volatile("s_nop 7" : "+v"(dq0), "+v"(dq1), "+v"(dk0), "+v"(dk1));
    asm volatile("s_nop 1" : "+v"(dv0), "+v"(dv1));
#pragma unroll
    for (int kk = 0; kk < 8; ++kk) {
      const lptr vs = L + ((kk + 4) & 7) * 16384 + w * 4096 + lane * 16;
      const v4u b0 = lds16(vs), b1 = lds16(vs + 1024), b2 = lds16(vs + 2048), b3 = lds16(vs + 3072);
      const v8u f0 = {b0.x, b0.y, b0.z, b0.w, b1.x, b1.y, b1.z, b1.w}, f1 = {b2.x, b2.y, b2.z, b2.w, b3.x, b3.y, b3.z, b3.w};
      SMF_VEC(dv0, A[kk], f0, idx);
      SMF_VEC(dv1, A[kk], f1, idx);
    }
    asm volatile("s_nop 7" : "+v"(dv0), "+v"(dv1));
    v4u qf, kf; v2u zv;
    {
      float zq[4], zk[4], zz[4];
#pragma unroll
      for (int r = 0; r < 4; ++r) { zq[r] = pairsum(dq0[r], dq1[r]); zk[r] = pairsum(dk0[r], dk1[r]); zz[r] = pairsum(dv0[r], dv1[r]); }
      const v2u aq = {pk2(zq[0], zq[1]), pk2(zq[2], zq[3])}, ak = {pk2(zk[0], zk[1]), pk2(zk[2], zk[3])};
      zv = v2u{pk2(zz[0], zz[1]), pk2(zz[2], zz[3])};
      f32x4 yq0, yq1, yk0, yk1;
      asm volatile("s_nop 1\n\tv_mfma_f32_16x16x16_bf16 %0, %4, %6, 0\n\tv_mfma_f32_16x16x16_bf16 %1, %4, %7, 0\n\t"
                   "v_mfma_f32_16x16x16_bf16 %2, %5, %6, 0\n\tv_mfma_f32_16x16x16_bf16 %3, %5, %7, 0\n\ts_nop 7"
                   : "=&v"(yq0), "=&v"(yq1), "=&v"(yk0), "=&v"(yk1) : "v"(aq), "v"(ak), "v"(sel0), "v"(sel1));
      qf = v4u{pk2(yq0[0], yq0[1]), pk2(yq0[2], yq0[3]), pk2(yq1[0], yq1[1]), pk2(yq1[2], yq1[3])};
      kf = v4u{pk2(yk0[0], yk0[1]), pk2(yk0[2], yk0[3]), pk2(yk1[0], yk1[1]), pk2(yk1[2], yk1[3])};
    }
    if (ch == 0) st16(kf, rs, OFF_KM + (s * 32 + h) * 4096 + w * 1024 + lane * 16);
    else st8(zv, rs, OFF_VM + (s * 32 + h) * 2048 + w * 512 + lane * 8);
    if (s == NSEG - 1) break;
    {
      f32x4 s0, s1, s2;
      asm volatile("s_nop 1\n\tv_mfma_f32_16x16x32_bf16 %0, %3, %6, 0\n\tv_mfma_f32_16x16x32_bf16 %1, %4, %6, 0\n\t"
                   "v_mfma_f32_16x16x32_bf16 %2, %5, %6, 0\n\ts_nop 7"
                   : "=&v"(s0), "=&v"(s1), "=&v"(s2) : "v"(kx[0]), "v"(kx[1]), "v"(kf), "v"(qf));
      lds16w(L + CH_SC + (w * 3 + 0) * 1024 + lane * 16, __builtin_bit_cast(v4u, s0));
      lds16w(L + CH_SC + (w * 3 + 1) * 1024 + lane * 16, __builtin_bit_cast(v4u, s1));
      lds16w(L + CH_SC + (w * 3 + 2) * 1024 + lane * 16, __builtin_bit_cast(v4u, s2));
    }
    BAR();
    f32x4 sx = {0.f, 0.f, 0.f, 0.f}, sm = sx;
#pragma unroll
    for (int ww = 0; ww < 4; ++ww) {
      sx += __builtin_bit_cast(f32x4, lds16(L + CH_SC + (ww * 3 + (n >> 3)) * 1024 + lane * 16));
      sm += __builtin_bit_cast(f32x4, lds16(L + CH_SC + (ww * 3 + 2) * 1024 + lane * 16));
    }
    float mx = fmaxf(fmaxf(fmaxf(sx[0], sx[1]), fmaxf(sx[2], sx[3])), fmaxf(fmaxf(sm[0], sm[1]), fmaxf(sm[2], sm[3])));
    mx = fmaxf(mx, xrow8(mx));
    mx = max32(max16(mx));
    const float cs = 0.08838834764831845f * 1.4426950408889634f;
    const float mc = mx * cs;
    float px[4], pm[4];
#pragma unroll
    for (int r = 0; r < 4; ++r) { px[r] = __builtin_amdgcn_exp2f(sx[r] * cs - mc); pm[r] = __builtin_amdgcn_exp2f(sm[r] * cs - mc); }
    const unsigned x01 = pk2(px[0], px[1]), x23 = pk2(px[2], px[3]), m01 = pk2(pm[0], pm[1]), m23 = pk2(pm[2], pm[3]);
    const bool lo = n < 8;
    const v4u pxf = {lo ? x01 : 0u, lo ? x23 : 0u, lo ? 0u : x01, lo ? 0u : x23};
    const bool c0 = lo && g < 2, c1 = lo && g >= 2;
    const v2u pm0 = {c0 ? m01 : 0u, c0 ? m23 : 0u}, pm1 = {c1 ? m01 : 0u, c1 ? m23 : 0u};
    float l = (px[0] + px[1]) + (px[2] + px[3]);
    if (c0) l += (pm[0] + pm[1]) + (pm[2] + pm[3]);
    l += xrow8(l);
    l = sum32(sum16(l));
    const float rl = 1.f / l;
    {
      f32x4 o0, o1;
      asm volatile("s_nop 1\n\tv_mfma_f32_16x16x32_bf16 %0, %2, %4, 0\n\tv_mfma_f32_16x16x32_bf16 %1, %3, %4, 0\n\t"
                   "v_mfma_f32_16x16x16_bf16 %0, %5, %6, %0\n\tv_mfma_f32_16x16x16_bf16 %1, %5, %7, %1\n\ts_nop 7"
                   : "=&v"(o0), "=&v"(o1) : "v"(vx[0]), "v"(vx[1]), "v"(pxf), "v"(zv), "v"(pm0), "v"(pm1));
      float e0[4], e1[4];
#pragma unroll
      for (int r = 0; r < 4; ++r) { e0[r] = (o0[r] + xrow8(o0[r])) * rl; e1[r] = (o1[r] + xrow8(o1[r])) * rl; }
      const int c8a = 4 * w + (g >> 1), c8b = c8a + 2;
      *(LAS v2u*)(L + CH_OA + (c8a >> 3) * 1024 + (c8a & 7) * 128 + (n & 7) * 16 + (g & 1) * 8) = v2u{pk2(e0[0], e0[1]), pk2(e0[2], e0[3])};
      *(LAS v2u*)(L + CH_OA + (c8b >> 3) * 1024 + (c8b & 7) * 128 + (n & 7) * 16 + (g & 1) * 8) = v2u{pk2(e1[0], e1[1]), pk2(e1[2], e1[3])};
    }
    BAR();
    {
      const v4u oa0 = lds16(L + CH_OA + lane * 16), oa1 = lds16(L + CH_OA + 1024 + lane * 16);
      f32x4 d[4];
#pragma unroll
      for (int t = 0; t < 4; ++t) d[t] = f32x4{0.f, 0.f, 0.f, 0.f};
      asm volatile("s_nop 1" : "+v"(d[0]), "+v"(d[1]), "+v"(d[2]), "+v"(d[3]));
#pragma unroll
      for (int t = 0; t < 4; ++t) SMF_VEC(d[t], oa0, wo[t][0], idx);
#pragma unroll
      for (int t = 0; t < 4; ++t) SMF_VEC(d[t], oa1, wo[t][1], idx);
      asm volatile("s_nop 7" : "+v"(d[0]), "+v"(d[1]), "+v"(d[2]), "+v"(d[3]));
      float za[4], zb[4];
#pragma unroll
      for (int t = 0; t < 4; ++t) { za[t] = pairsum(d[t][0], d[t][2]); zb[t] = pairsum(d[t][1], d[t][3]); }
      const v4u o = {nomark(pk2(za[0], za[1])), nomark(pk2(za[2], za[3])), nomark(pk2(zb[0], zb[1])), nomark(pk2(zb[2], zb[3]))};
      const int j = 32 * ch + 8 * w + (n >> 1);
      st16(o, rs, rbase + ((s & 1) * 32 + (j >> 1)) * 16384 + (j & 1) * 4096 + h * 128 + (2 * g + (n & 1)) * 16);
    }
    if (w == 3) {
      const int roff = rbase + ((s & 1) * 32 + (c >> 1)) * 16384 + (c & 1) * 4096 + lane * 16;
      v4u e[4];
      unsigned spins = 0;
      for (;;) {
        asm volatile("" ::: "memory");
#pragma unroll
        for (int k = 0; k < 4; ++k) e[k] = ld16(rs, roff + k * 1024);
        if (!__any(has_mark(e[0]) || has_mark(e[1]) || has_mark(e[2]) || has_mark(e[3]))) break;
        if (spin_fail(spins, rs, 0x300u + s)) break;
      }
      f32x4 d;
      asm volatile("s_nop 1\n\tv_mfma_f32_16x16x32_bf16 %0, %1, %2, 0\n\tv_mfma_f32_16x16x32_bf16 %0, %1, %3, %0\n\t"
                   "v_mfma_f32_16x16x32_bf16 %0, %1, %4, %0\n\tv_mfma_f32_16x16x32_bf16 %0, %1, %5, %0\n\ts_nop 7"
                   : "=&v"(d) : "v"(selr), "v"(e[0]), "v"(e[1]), "v"(e[2]), "v"(e[3]));
#pragma unroll
      for (int r = 0; r < 4; ++r) d[r] += xrow8(d[r]);
      const unsigned u0 = nomark(pk2(d[0], d[1])), u1 = nomark(pk2(d[2], d[3]));
      const unsigned v0 = uq1(u0), v1 = uq1(u1);
      const int gq = (n & 7) >> 1;
      const int row = 4 * (gq & 1) + 2 * (gq >> 1) + g;
      if (n < 8 && (n & 1) == 0 && g < 2) st16(v4u{u0, u1, v0, v1}, rs, gbase + (s + 1) * 16384 + c * 128 + row * 16);
      const v4u mk = {MARK, MARK, MARK, MARK};
#pragma unroll
      for (int k = 0; k < 4; ++k) st16(mk, rs, roff + k * 1024);
    } else {
      WAIT_VM(0);
    }
    BAR();
    if (tid == 0) st4((unsigned)(s + 1), rs, OFF_FLAG + (FL_KM + c) * 128);
  }
  WAIT_VM(0);
  BAR();
  if (tid == 0) st4((unsigned)NSEG, rs, OFF_FLAG + (FL_KM + c) * 128);
}

DEV void stage_x(const unsigned char* src, unsigned dst, int w, int lane) {
#pragma unroll
  for (int q = 0; q < 16; ++q) glds(src, (w * 16 + q) * 1024 + lane * 16, dst + (w * 16 + q) * 1024);
}

DEV void role_qkv(const Params& p, int nq, lptr L) {
  const int tid = threadIdx.x, lane = tid & 63, w = __builtin_amdgcn_readfirstlane(tid >> 6);
  const int type = nq >> 5, h = nq & 31;
  const int g = lane >> 4, q = (lane >> 2) & 3, pp = lane & 3;
  const rsrc_t rs = make_rsrc(p.ws, WS_END);
  const unsigned Lb = (unsigned)(size_t)L;
  const unsigned short* W = (type == 0 ? p.wk : type == 1 ? p.wv : p.wq) + h * 128;
  sfor<0, 8>([&](auto I) { stage_chunk(W + (size_t)(64 * I) * 4096, 8192, Lb + I * 16384, w, lane); });
  WAIT_VM(0);
  BAR();
  v4u wf[16][2];
#pragma unroll
  for (int kk = 0; kk < 16; ++kk)
#pragma unroll
    for (int t = 0; t < 2; ++t) {
      const lptr slot = L + (kk >> 1) * 16384;
      const unsigned c4 = type == 1 ? 8 * w + 4 * t + pp : 8 * w + 2 * pp + t;
      const v2u a = trfrag(slot, 32 * (kk & 1) + 8 * g + q, c4), b = trfrag(slot, 32 * (kk & 1) + 8 * g + 4 + q, c4);
      wf[kk][t] = v4u{a.x, a.y, b.x, b.y};
    }
  BAR();
  const unsigned char* xs = p.ws + OFF_XN;
  const int obase = (type == 0 ? OFF_K : type == 1 ? OFF_V : OFF_Q) + h * 8192 + lane * 16;
  const int foff = OFF_FLAG + (FL_QKV + nq) * 128;
  stage_x(xs, Lb, w, lane);
#pragma unroll 1
  for (int i = 0; i < 32; ++i) {
    if (i == 0) WAIT_VM(0); else WAIT_VM(4);
    BAR();
    if (i >= 2 && tid == 0) st4((unsigned)(i - 1), rs, foff);
    if (i + 1 < 32) stage_x(xs + (i + 1) * 65536, Lb + ((i + 1) & 1) * 65536, w, lane);
    const lptr X = L + (i & 1) * 65536 + lane * 16;
    f32x4 acc[4][2];
#pragma unroll
    for (int T = 0; T < 4; ++T) { acc[T][0] = f32x4{0.f, 0.f, 0.f, 0.f}; acc[T][1] = acc[T][0]; }
    if (type == 1) {
#pragma unroll
      for (int kk = 0; kk < 16; ++kk)
#pragma unroll
        for (int T = 0; T < 4; ++T) {
          const v4u xf = lds16(X + (T * 16 + kk) * 1024);
          acc[T][0] = mfma32(xf, wf[kk][0], acc[T][0]);
          acc[T][1] = mfma32(xf, wf[kk][1], acc[T][1]);
        }
#pragma unroll
      for (int sg = 0; sg < 2; ++sg)
#pragma unroll
        for (int t = 0; t < 2; ++t) {
          const f32x4 a = acc[2 * sg][t], b = acc[2 * sg + 1][t];
          st16(v4u{pk2(a[0], a[1]), pk2(a[2], a[3]), pk2(b[0], b[1]), pk2(b[2], b[3])}, rs, obase + (2 * i + sg) * 262144 + (2 * w + t) * 1024);
        }
    } else {
#pragma unroll
      for (int kk = 0; kk < 16; ++kk)
#pragma unroll
        for (int T = 0; T < 4; ++T) {
          const v4u xf = lds16(X + (T * 16 + kk) * 1024);
          acc[T][0] = mfma32(wf[kk][0], xf, acc[T][0]);
          acc[T][1] = mfma32(wf[kk][1], xf, acc[T][1]);
        }
#pragma unroll
      for (int T = 0; T < 4; ++T) {
        const f32x4 a = acc[T][0], b = acc[T][1];
        st16(v4u{pk2(a[0], a[1]), pk2(a[2], a[3]), pk2(b[0], b[1]), pk2(b[2], b[3])}, rs, obase + (2 * i + (T >> 1)) * 262144 + ((T & 1) * 4 + w) * 1024);
      }
    }
    if (i == 0) {
      WAIT_VM(0);
      BAR();
      if (tid == 0) st4(1u, rs, foff);
    }
  }
  WAIT_VM(0);
  BAR();
  if (tid == 0) st4(32u, rs, foff);
}

#define MFMA_ACC(acc, a, b) asm volatile("v_mfma_f32_16x16x32_bf16 %0, %1, %2, %0" : "+v"(acc) : "a"(a), "v"(b))
DEV v4u pin_acc4(v4u x) { v4u y; asm volatile("" : "=a"(y) : "0"(x)); return y; }
DEV float gelu_tanh(float x) {
  const float u = 0.7978845608028654f * (x + 0.044715f * x * x * x);
  return x / (1.f + __builtin_amdgcn_exp2f(-2.f * 1.4426950408889634f * u));
}
DEV void role_mlp(const Params& p, int m, lptr L) {
  const int tid = threadIdx.x, lane = tid & 63, w = __builtin_amdgcn_readfirstlane(tid >> 6);
  const int grp = m / 14, j = m % 14;
  const int g = lane >> 4, q = (lane >> 2) & 3, pp = lane & 3;
  const rsrc_t rs = make_rsrc(p.ws, WS_END);
  const unsigned Lb = (unsigned)(size_t)L;
  v4u w1f[16][2], w2f[4][2][4];
  sfor<0, 8>([&](auto I) { stage_chunk(p.w1 + (size_t)(64 * I) * 1792 + 128 * j, 3584, Lb + I * 16384, w, lane); });
  WAIT_VM(0);
  BAR();
#pragma unroll
  for (int kk = 0; kk < 16; ++kk)
#pragma unroll
    for (int t = 0; t < 2; ++t) {
      const lptr slot = L + (kk >> 1) * 16384;
      const v2u a = trfrag(slot, 32 * (kk & 1) + 8 * g + q, 8 * w + 2 * pp + t), b = trfrag(slot, 32 * (kk & 1) + 8 * g + 4 + q, 8 * w + 2 * pp + t);
      w1f[kk][t] = pin_acc4(v4u{a.x, a.y, b.x, b.y});
    }
  BAR();
  sfor<0, 8>([&](auto I) { stage_chunk(p.w2 + (size_t)(128 * j + 64 * (I >> 2)) * 512 + 128 * (I & 3), 1024, Lb + I * 16384, w, lane); });
  WAIT_VM(0);
  BAR();
#pragma unroll
  for (int cg = 0; cg < 4; ++cg)
#pragma unroll
    for (int t = 0; t < 2; ++t)
#pragma unroll
      for (int kk = 0; kk < 4; ++kk) {
        const lptr slot = L + ((kk >> 1) * 4 + w) * 16384;
        const v2u a = trfrag(slot, 32 * (kk & 1) + 8 * g + q, 8 * cg + 2 * pp + t), b = trfrag(slot, 32 * (kk & 1) + 8 * g + 4 + q, 8 * cg + 2 * pp + t);
        w2f[cg][t][kk] = pin_acc4(v4u{a.x, a.y, b.x, b.y});
      }
  BAR();
  float bias[8];
  {
    const v4u bv = *(const GAS v4u*)((const GAS unsigned short*)p.b1 + 128 * j + 32 * w + 8 * g);
#pragma unroll
    for (int k = 0; k < 4; ++k) { bias[2 * k] = lo16(bv[k]); bias[2 * k + 1] = hi16(bv[k]); }
    asm volatile("s_waitcnt vmcnt(0)" : "+v"(bias[0]), "+v"(bias[1]), "+v"(bias[2]), "+v"(bias[3]), "+v"(bias[4]), "+v"(bias[5]), "+v"(bias[6]), "+v"(bias[7])::"memory");
  }
  const unsigned char* ys = p.ws + OFF_Y;
  const lptr HX = L + 131072;
  const int foff = OFF_FLAG + (FL_MLP + m) * 128;
  stage_x(ys + grp * 65536, Lb, w, lane);
#pragma unroll 1
  for (int it = 0; it < 16; ++it) {
    const int i = 2 * it + grp;
    if (it == 0) WAIT_VM(0); else WAIT_VM(16);
    BAR();
    if (it >= 1 && tid == 0) st4((unsigned)(it - 1 > 0 ? it - 1 : 0), rs, foff);
    if (it + 1 < 16) stage_x(ys + (i + 2) * 65536, Lb + ((it + 1) & 1) * 65536, w, lane);
    const lptr X = L + (it & 1) * 65536 + lane * 16;
    {
      f32x4 acc[4][2];
#pragma unroll
      for (int T = 0; T < 4; ++T) { acc[T][0] = f32x4{0.f, 0.f, 0.f, 0.f}; acc[T][1] = acc[T][0]; }
      asm volatile("s_nop 1" : "+v"(acc[0][0]), "+v"(acc[0][1]), "+v"(acc[1][0]), "+v"(acc[1][1]), "+v"(acc[2][0]), "+v"(acc[2][1]), "+v"(acc[3][0]), "+v"(acc[3][1]));
#pragma unroll
      for (int kk = 0; kk < 16; ++kk)
#pragma unroll
        for (int T = 0; T < 4; ++T) {
          const v4u xf = lds16(X + (T * 16 + kk) * 1024);
          MFMA_ACC(acc[T][0], w1f[kk][0], xf);
          MFMA_ACC(acc[T][1], w1f[kk][1], xf);
        }
      asm volatile("s_nop 7" : "+v"(acc[0][0]), "+v"(acc[0][1]), "+v"(acc[1][0]), "+v"(acc[1][1]), "+v"(acc[2][0]), "+v"(acc[2][1]), "+v"(acc[3][0]), "+v"(acc[3][1]));
#pragma unroll
      for (int T = 0; T < 4; ++T) {
        float hv[8];
#pragma unroll
        for (int r = 0; r < 4; ++r) {
          hv[r] = gelu_tanh(rbf(rbf(acc[T][0][r]) + bias[r]));
          hv[4 + r] = gelu_tanh(rbf(rbf(acc[T][1][r]) + bias[4 + r]));
        }
        lds16w(HX + (T * 4 + w) * 1024 + lane * 16, v4u{pk2(hv[0], hv[1]), pk2(hv[2], hv[3]), pk2(hv[4], hv[5]), pk2(hv[6], hv[7])});
      }
    }
    BAR();
#pragma unroll
    for (int tp = 0; tp < 2; ++tp) {
      f32x4 acc[2][4][2];
#pragma unroll
      for (int T = 0; T < 2; ++T)
#pragma unroll
        for (int cg = 0; cg < 4; ++cg) { acc[T][cg][0] = f32x4{0.f, 0.f, 0.f, 0.f}; acc[T][cg][1] = acc[T][cg][0]; }
#pragma unroll
      for (int T = 0; T < 2; ++T)
        asm volatile("s_nop 1" : "+v"(acc[T][0][0]), "+v"(acc[T][0][1]), "+v"(acc[T][1][0]), "+v"(acc[T][1][1]), "+v"(acc[T][2][0]), "+v"(acc[T][2][1]), "+v"(acc[T][3][0]), "+v"(acc[T][3][1]));
#pragma unroll
      for (int kk = 0; kk < 4; ++kk)
#pragma unroll
        for (int T = 0; T < 2; ++T) {
          const v4u hf = lds16(HX + ((2 * tp + T) * 4 + kk) * 1024 + lane * 16);
#pragma unroll
          for (int cg = 0; cg < 4; ++cg) { MFMA_ACC(acc[T][cg][0], w2f[cg][0][kk], hf); MFMA_ACC(acc[T][cg][1], w2f[cg][1][kk], hf); }
        }
#pragma unroll
      for (int T = 0; T < 2; ++T)
        asm volatile("s_nop 7" : "+v"(acc[T][0][0]), "+v"(acc[T][0][1]), "+v"(acc[T][1][0]), "+v"(acc[T][1][1]), "+v"(acc[T][2][0]), "+v"(acc[T][2][1]), "+v"(acc[T][3][0]), "+v"(acc[T][3][1]));
      const int pbase = OFF_PART + ((2 * i + tp) * NPART + 32 + j) * 32768 + lane * 16;
#pragma unroll
      for (int T = 0; T < 2; ++T)
#pragma unroll
        for (int cg = 0; cg < 4; ++cg) {
          const f32x4 a = acc[T][cg][0], b = acc[T][cg][1];
          st16(v4u{pk2(a[0], a[1]), pk2(a[2], a[3]), pk2(b[0], b[1]), pk2(b[2], b[3])}, rs, pbase + ((4 * w + cg) * 2 + T) * 1024);
        }
    }
  }
  WAIT_VM(0);
  BAR();
  if (tid == 0) st4(16u, rs, foff);
}

constexpr int FA_Q = 0, FA_K = 8192, FA_V = 16384, FA_KM = 24576, FA_VM = 28672, FA_O = 30720;
DEV void role_fatt(const Params& p, int h, lptr L0) {
  const int tid = threadIdx.x, lane = tid & 63, w = __builtin_amdgcn_readfirstlane(tid >> 6);
  const int n = lane & 15, g = lane >> 4, q = (lane >> 2) & 3, pp = lane & 3;
  const rsrc_t rs = make_rsrc(p.ws, WS_END);
  const unsigned Lb = (unsigned)(size_t)L0;
  sfor<0, 8>([&](auto I) { stage_chunk(p.wo + (size_t)(h * 128 + 64 * (I >> 2)) * 512 + 128 * (I & 3), 1024, Lb + I * 16384, w, lane); });
  WAIT_VM(0);
  BAR();
  v4u wf[4][2][4];
#pragma unroll
  for (int cg = 0; cg < 4; ++cg)
#pragma unroll
    for (int t = 0; t < 2; ++t)
#pragma unroll
      for (int kk = 0; kk < 4; ++kk) {
        const lptr slot = L0 + ((kk >> 1) * 4 + w) * 16384;
        const v2u a = trfrag(slot, 32 * (kk & 1) + 4 * g + q, 8 * cg + 2 * pp + t), b = trfrag(slot, 32 * (kk & 1) + 16 + 4 * g + q, 8 * cg + 2 * pp + t);
        wf[cg][t][kk] = v4u{a.x, a.y, b.x, b.y};
      }
  BAR();
  const lptr L = L0;
  const int T = w & 1, eh = w >> 1;
  const int qkvflag = OFF_FLAG + (FL_QKV + (lane % 3) * 32 + h) * 128;
  const int kmflag = OFF_FLAG + (FL_KM + 2 * h + (lane & 1)) * 128;
  const float cs = 0.08838834764831845f * 1.4426950408889634f;
#pragma unroll 1
  for (int s = 0; s < NSEG; ++s) {
    wait_flags(rs, qkvflag, (unsigned)(s >> 1) + 1, 0x400u + s);
    v4u t0[6];
    const int tb = (s * 32 + h) * 8192 + lane * 16;
#pragma unroll
    for (int k = 0; k < 2; ++k) {
      t0[k] = ld16(rs, OFF_Q + tb + (2 * w + k) * 1024);
      t0[2 + k] = ld16(rs, OFF_K + tb + (2 * w + k) * 1024);
      t0[4 + k] = ld16(rs, OFF_V + tb + (2 * w + k) * 1024);
    }
    wait_flags(rs, kmflag, (unsigned)s + 1, 0x500u + s);
    const v4u km = ld16(rs, OFF_KM + (s * 32 + h) * 4096 + w * 1024 + lane * 16);
    const v2u vm = ld8(rs, OFF_VM + (s * 32 + h) * 2048 + w * 512 + lane * 8);
#pragma unroll
    for (int k = 0; k < 2; ++k) {
      lds16w(L + FA_Q + (2 * w + k) * 1024 + lane * 16, t0[k]);
      lds16w(L + FA_K + (2 * w + k) * 1024 + lane * 16, t0[2 + k]);
      lds16w(L + FA_V + (2 * w + k) * 1024 + lane * 16, t0[4 + k]);
    }
    lds16w(L + FA_KM + w * 1024 + lane * 16, km);
    *(LAS v2u*)(L + FA_VM + w * 512 + lane * 8) = vm;
    BAR();
    f32x4 s0 = {0.f, 0.f, 0.f, 0.f}, s1 = s0, s2 = s0;
#pragma unroll
    for (int kk = 0; kk < 4; ++kk) {
      const v4u qf = lds16(L + FA_Q + (T * 4 + kk) * 1024 + lane * 16);
      s0 = mfma32(lds16(L + FA_K + kk * 1024 + lane * 16), qf, s0);
      s1 = mfma32(lds16(L + FA_K + (4 + kk) * 1024 + lane * 16), qf, s1);
      s2 = mfma32(lds16(L + FA_KM + kk * 1024 + lane * 16), qf, s2);
    }
    const int tok = 16 * T + n;
    const float ninf = -__builtin_inff();
    float mx = fmaxf(fmaxf(s2[0], s2[1]), fmaxf(s2[2], s2[3]));
#pragma unroll
    for (int r = 0; r < 4; ++r) {
      s0[r] = (4 * g + r <= tok) ? s0[r] : ninf;
      s1[r] = (16 + 4 * g + r <= tok) ? s1[r] : ninf;
      mx = fmaxf(mx, fmaxf(s0[r], s1[r]));
    }
    mx = max32(max16(mx));
    const float mc = mx * cs;
    float p0[4], p1[4], p2[4];
#pragma unroll
    for (int r = 0; r < 4; ++r) {
      p0[r] = __builtin_amdgcn_exp2f(s0[r] * cs - mc);
      p1[r] = __builtin_amdgcn_exp2f(s1[r] * cs - mc);
      p2[r] = __builtin_amdgcn_exp2f(s2[r] * cs - mc);
    }
    float l = ((p0[0] + p0[1]) + (p0[2] + p0[3])) + ((p1[0] + p1[1]) + (p1[2] + p1[3]));
    if (g < 2) l += (p2[0] + p2[1]) + (p2[2] + p2[3]);
    l = sum32(sum16(l));
    const float rl = 1.f / l;
    const v4u pxf = {pk2(p0[0], p0[1]), pk2(p0[2], p0[3]), pk2(p1[0], p1[1]), pk2(p1[2], p1[3])};
    const unsigned m01 = pk2(p2[0], p2[1]), m23 = pk2(p2[2], p2[3]);
    const v2u pm0 = {g < 2 ? m01 : 0u, g < 2 ? m23 : 0u}, pm1 = {g < 2 ? 0u : m01, g < 2 ? 0u : m23};
#pragma unroll
    for (int k = 0; k < 2; ++k) {
      const int kk = 2 * eh + k;
      const f32x4 z4 = {0.f, 0.f, 0.f, 0.f};
      const v2u zv = *(LAS v2u*)(L + FA_VM + kk * 512 + lane * 8);
      f32x4 o0 = mfma32(lds16(L + FA_V + (2 * kk) * 1024 + lane * 16), pxf, z4);
      f32x4 o1 = mfma32(lds16(L + FA_V + (2 * kk + 1) * 1024 + lane * 16), pxf, z4);
      o0 = mfma16(zv, pm0, o0);
      o1 = mfma16(zv, pm1, o1);
      lds16w(L + FA_O + (T * 4 + kk) * 1024 + lane * 16,
             v4u{pk2(o0[0] * rl, o0[1] * rl), pk2(o0[2] * rl, o0[3] * rl), pk2(o1[0] * rl, o1[1] * rl), pk2(o1[2] * rl, o1[3] * rl)});
    }
    BAR();
    const int pbase = OFF_PART + (s * NPART + h) * 32768 + lane * 16;
#pragma unroll
    for (int TT = 0; TT < 2; ++TT) {
      f32x4 acc[4][2];
#pragma unroll
      for (int cg = 0; cg < 4; ++cg) { acc[cg][0] = f32x4{0.f, 0.f, 0.f, 0.f}; acc[cg][1] = acc[cg][0]; }
#pragma unroll
      for (int kk = 0; kk < 4; ++kk) {
        const v4u of = lds16(L + FA_O + (TT * 4 + kk) * 1024 + lane * 16);
#pragma unroll
        for (int cg = 0; cg < 4; ++cg) { acc[cg][0] = mfma32(wf[cg][0][kk], of, acc[cg][0]); acc[cg][1] = mfma32(wf[cg][1][kk], of, acc[cg][1]); }
      }
#pragma unroll
      for (int cg = 0; cg < 4; ++cg) {
        const f32x4 a = acc[cg][0], b = acc[cg][1];
        st16(v4u{pk2(a[0], a[1]), pk2(a[2], a[3]), pk2(b[0], b[1]), pk2(b[2], b[3])}, rs, pbase + ((4 * w + cg) * 2 + TT) * 1024);
      }
    }
    WAIT_VM(0);
    BAR();
    if (tid == 0) st4((unsigned)s + 1, rs, OFF_FLAG + (FL_F + h) * 128);
  }
}

DEV void role_red(const Params& p, int u, lptr L) {
  const int tid = threadIdx.x, lane = tid & 63, w = __builtin_amdgcn_readfirstlane(tid >> 6);
  const int n = lane & 15, g = lane >> 4, cg = u >> 1, T = u & 1;
  const rsrc_t rs = make_rsrc(p.ws, WS_END);
  const v4u bv = *(const GAS v4u*)((const GAS unsigned short*)p.b2 + 32 * cg + 8 * g);
  const int fflag = OFF_FLAG + (FL_F + (lane & 31)) * 128;
#pragma unroll 1
  for (int s = 0; s < NSEG; ++s) {
    const int i = s >> 1;
    const size_t xo = (size_t)(32 * s + 16 * T + n) * 512 + 32 * cg + 8 * g;
    wait_flags(rs, OFF_FLAG + (FL_MLP + (i & 1) * 14 + (lane % 14)) * 128, (unsigned)(i >> 1) + 1, 0x600u + s);
    wait_flags(rs, fflag, (unsigned)s + 1, 0x700u + s);
    float a[8];
#pragma unroll
    for (int k = 0; k < 8; ++k) a[k] = 0.f;
    const int pb = OFF_PART + s * NPART * 32768 + u * 1024 + lane * 16;
    v4u e[12];
#pragma unroll
    for (int k = 0; k < 12; ++k) { const int pi = w + 4 * k; e[k] = ld16(rs, pb + (pi < NPART ? pi : 0) * 32768); }
#pragma unroll
    for (int k = 0; k < 12; ++k) {
      const bool on = w + 4 * k < NPART;
#pragma unroll
      for (int jj = 0; jj < 4; ++jj) { a[2 * jj] += on ? lo16(e[k][jj]) : 0.f; a[2 * jj + 1] += on ? hi16(e[k][jj]) : 0.f; }
    }
    *(LAS f32x4*)(L + (w * 2 + 0) * 1024 + lane * 16) = f32x4{a[0], a[1], a[2], a[3]};
    *(LAS f32x4*)(L + (w * 2 + 1) * 1024 + lane * 16) = f32x4{a[4], a[5], a[6], a[7]};
    BAR();
    if (w == 0) {
      const v4u xv = *(const GAS v4u*)((const GAS unsigned short*)p.x + xo);
      f32x4 s0 = {0.f, 0.f, 0.f, 0.f}, s1 = s0;
#pragma unroll
      for (int ww = 0; ww < 4; ++ww) { s0 += *(LAS f32x4*)(L + (ww * 2 + 0) * 1024 + lane * 16); s1 += *(LAS f32x4*)(L + (ww * 2 + 1) * 1024 + lane * 16); }
      v4u o;
      o.x = pk2(s0[0] + lo16(xv.x) + lo16(bv.x), s0[1] + hi16(xv.x) + hi16(bv.x));
      o.y = pk2(s0[2] + lo16(xv.y) + lo16(bv.y), s0[3] + hi16(xv.y) + hi16(bv.y));
      o.z = pk2(s1[0] + lo16(xv.z) + lo16(bv.z), s1[1] + hi16(xv.z) + hi16(bv.z));
      o.w = pk2(s1[2] + lo16(xv.w) + lo16(bv.w), s1[3] + hi16(xv.w) + hi16(bv.w));
      *(GAS v4u*)((GAS unsigned short*)p.out + xo) = o;
    }
    BAR();
  }
}

__global__ void __launch_bounds__(256, 1) k_main(Params p) {
  extern __shared__ __attribute__((aligned(16))) unsigned char smem[];
  const lptr L = (lptr)smem;
  const int b = blockIdx.x, lo = b & 7, id = (b >> 3) * 4 + (lo & 3);
  if (lo < 4) {
    if (id < 64) role_chain(p, id, L);
    else if (id < 96) role_fatt(p, id - 64, L);
    else role_red(p, id - 96, L);
  } else {
    if (id < 96) role_qkv(p, id, L);
    else if (id < 124) role_mlp(p, id - 96, L);
  }
}

}

extern "C" void kernel_launch(void* const* d_in, const int* in_sizes, int n_in, void* d_out, int out_size, void* d_ws, size_t ws_size,
                              hipStream_t stream) {
  Params p;
  p.x = (const unsigned short*)d_in[0];
  p.mem0 = (const unsigned short*)d_in[1];
  p.wq = (const unsigned short*)d_in[2];
  p.wk = (const unsigned short*)d_in[3];
  p.wv = (const unsigned short*)d_in[4];
  p.wo = (const unsigned short*)d_in[5];
  p.w1 = (const unsigned short*)d_in[8];
  p.b1 = (const unsigned short*)d_in[9];
  p.w2 = (const unsigned short*)d_in[10];
  p.b2 = (const unsigned short*)d_in[11];
  p.out = (unsigned short*)d_out;
  const uintptr_t a = ((uintptr_t)d_ws + (2u << 20) - 1) & ~(uintptr_t)((2u << 20) - 1);
  p.ws = (unsigned char*)a;
  hipFuncSetAttribute((const void*)k_main, hipFuncAttributeMaxDynamicSharedMemorySize, LDS_BYTES);
  k_prep<<<256, 256, 0, stream>>>(p);
  k_main<<<256, 256, LDS_BYTES, stream>>>(p);
}
```

```cpp
#include <hip/hip_runtime.h>
#include <hip/hip_bf16.h>
#include <stdint.h>

#define GAS __attribute__((address_space(1)))
#define LAS __attribute__((address_space(3)))
#define DEV __device__ __forceinline__
typedef unsigned v4u __attribute__((ext_vector_type(4)));
typedef unsigned v2u __attribute__((ext_vector_type(2)));
typedef unsigned v8u __attribute__((ext_vector_type(8)));
typedef float f32x4 __attribute__((ext_vector_type(4)));
typedef __bf16 bf16x2 __attribute__((ext_vector_type(2)));
typedef __bf16 bf16x8 __attribute__((ext_vector_type(8)));
typedef __bf16 bf16x16 __attribute__((ext_vector_type(16)));
typedef short s16x4 __attribute__((ext_vector_type(4)));
typedef LAS unsigned char* lptr;

namespace {

constexpr int NSEG = 64;
constexpr int MB = 1 << 20;
constexpr int OFF_G = 0;
constexpr int OFF_R = 2 * MB;
constexpr int OFF_FLAG = 4 * MB;
constexpr int OFF_XN = 5 * MB;
constexpr int OFF_Y = 7 * MB;
constexpr int OFF_Q = 16 * MB;
constexpr int OFF_K = 32 * MB;
constexpr int OFF_V = 48 * MB;
constexpr int OFF_KM = 64 * MB;
constexpr int OFF_VM = 72 * MB;
constexpr int OFF_PART = 80 * MB;
constexpr int NPART = 46;
constexpr int WS_END = OFF_PART + NSEG * NPART * 32768;
constexpr int FL_ABORT = 0, FL_QKV = 8, FL_MLP = 104, FL_FT = 136, FL_F = 200, FL_END = 240;
constexpr int OFF_KMF = OFF_G + 65 * 16384;
constexpr int OFF_PAR = OFF_FLAG + 31744;
constexpr int NPROBE = 16;
constexpr int OFF_DBG = OFF_FLAG + 32768;
constexpr int OFF_DBG2 = 9 * MB;
constexpr unsigned MARK = 0xFFFFFFFFu;
#define T_D1 84
#define T_D2 132
#define T_DX 20
#define T_HOLD 550
#define CODE_PREFETCH 1
constexpr int CODE_KIB = 70;
#define T_PROBE 0
#define WARM 0
#define T_HOLD_TAIL 1500
#define T_HOLD_KV 0
constexpr unsigned SPIN_CAP = 1u << 17;
constexpr int AUX_SC1 = 16;
constexpr int LDS_BYTES = 160 * 1024;

struct Params {
  const unsigned short *x, *mem0, *wq, *wk, *wv, *wo, *w1, *b1, *w2, *b2;
  unsigned short* out;
  unsigned char* ws;
  int d1, d2, fb, unused;
};

typedef __amdgpu_buffer_rsrc_t rsrc_t;
DEV rsrc_t make_rsrc(const void* p, int bytes) { return __builtin_amdgcn_make_buffer_rsrc((void*)p, 0, bytes, 0x00020000); }
DEV v4u ld16(rsrc_t r, int off) { return __builtin_amdgcn_raw_buffer_load_b128(r, off, 0, AUX_SC1); }
DEV v2u ld8(rsrc_t r, int off) { return __builtin_amdgcn_raw_buffer_load_b64(r, off, 0, AUX_SC1); }
DEV unsigned ld4(rsrc_t r, int off) { return __builtin_amdgcn_raw_buffer_load_b32(r, off, 0, AUX_SC1); }
DEV void st16(v4u v, rsrc_t r, int off) { __builtin_amdgcn_raw_buffer_store_b128(v, r, off, 0, AUX_SC1); }
DEV void st8(v2u v, rsrc_t r, int off) { __builtin_amdgcn_raw_buffer_store_b64(v, r, off, 0, AUX_SC1); }
DEV void st4(unsigned v, rsrc_t r, int off) { __builtin_amdgcn_raw_buffer_store_b32(v, r, off, 0, AUX_SC1); }

DEV float lo16(unsigned u) { return __builtin_bit_cast(float, u << 16); }
DEV float hi16(unsigned u) { return __builtin_bit_cast(float, u & 0xffff0000u); }
DEV unsigned pk2(float a, float b) { bf16x2 v = {(__bf16)a, (__bf16)b}; return __builtin_bit_cast(unsigned, v); }
DEV float rbf(float f) { return (float)(__bf16)f; }
DEV unsigned nomark(unsigned u) { return u < 0xFFFFFFFEu ? u : 0xFFFFFFFEu; }

DEV f32x4 mfma32(v4u a, v4u b, f32x4 c) {
  return __builtin_amdgcn_mfma_f32_16x16x32_bf16(__builtin_bit_cast(bf16x8, a), __builtin_bit_cast(bf16x8, b), c, 0, 0, 0);
}
DEV f32x4 mfma16(v2u a, v2u b, f32x4 c) {
  return __builtin_amdgcn_mfma_f32_16x16x16bf16_1k(__builtin_bit_cast(s16x4, a), __builtin_bit_cast(s16x4, b), c, 0, 0, 0);
}
DEV f32x4 smf(v4u a, v8u b, f32x4 c, int idx) {
  return __builtin_amdgcn_smfmac_f32_16x16x64_bf16(__builtin_bit_cast(bf16x8, a), __builtin_bit_cast(bf16x16, b), c, idx, 0, 0);
}
#define BAR() asm volatile("s_waitcnt lgkmcnt(0)\n\ts_barrier" ::: "memory")
#define SB() __builtin_amdgcn_sched_barrier(0)
#define WAIT_VM(n) asm volatile("s_waitcnt vmcnt(%0)" ::"n"(n) : "memory")
DEV v4u lds16(lptr p) { return *(LAS v4u*)p; }
DEV void lds16w(lptr p, v4u v) { *(LAS v4u*)p = v; }
DEV v2u trread(lptr p) {
  return __builtin_bit_cast(v2u, __builtin_amdgcn_ds_read_tr16_b64_v4i16((LAS s16x4*)p));
}
DEV float xrow8(float v) { return __builtin_bit_cast(float, __builtin_amdgcn_update_dpp(0, __builtin_bit_cast(int, v), 0x128, 0xf, 0xf, true)); }
DEV unsigned uq1(unsigned v) { return (unsigned)__builtin_amdgcn_update_dpp(0, (int)v, 0xB1, 0xf, 0xf, true); }
DEV float asf(unsigned u) { return __builtin_bit_cast(float, u); }
DEV unsigned asu(float f) { return __builtin_bit_cast(unsigned, f); }
DEV float max16(float v) { auto r = __builtin_amdgcn_permlane16_swap(asu(v), asu(v), false, false); const unsigned a = r[0], b = r[1]; return fmaxf(asf(a), asf(b)); }
DEV float max32(float v) { auto r = __builtin_amdgcn_permlane32_swap(asu(v), asu(v), false, false); const unsigned a = r[0], b = r[1]; return fmaxf(asf(a), asf(b)); }
DEV float sum16(float v) { auto r = __builtin_amdgcn_permlane16_swap(asu(v), asu(v), false, false); const unsigned a = r[0], b = r[1]; return asf(a) + asf(b); }
DEV float sum32(float v) { auto r = __builtin_amdgcn_permlane32_swap(asu(v), asu(v), false, false); const unsigned a = r[0], b = r[1]; return asf(a) + asf(b); }
DEV float pairsum(float x, float y) { auto r = __builtin_amdgcn_permlane32_swap(asu(x), asu(y), false, false); const unsigned a = r[0], b = r[1]; return asf(a) + asf(b); }

template <int I, int N, class F> DEV void sfor(F&& f) { if constexpr (I < N) { f(std::integral_constant<int, I>{}); sfor<I + 1, N>(f); } }

DEV void glds(const void* base, unsigned voff, unsigned dst) {
  unsigned keep;
  asm volatile("s_mov_b32 %0, m0\n\ts_mov_b32 m0, %3\n\ts_nop 0\n\tglobal_load_lds_dwordx4 %1, %2\n\ts_mov_b32 m0, %0"
               : "=&s"(keep) : "v"(voff), "s"(base), "s"(dst) : "memory");
}
DEV unsigned swz(unsigned row) { return ((row & 3) << 2) | ((row >> 2) & 3); }
DEV void stage_chunk(const void* base, unsigned stride, unsigned slot, int w, int lane) {
#pragma unroll
  for (int q = 0; q < 4; ++q) {
    const unsigned ii = 4 * w + q, row = 4 * ii + (lane >> 4);
    glds(base, row * stride + (((lane & 15) ^ swz(row)) << 4), slot + ii * 1024);
  }
}
DEV v2u trfrag(lptr slot, unsigned row, unsigned c4) { return trread(slot + 256 * row + 16 * ((c4 >> 1) ^ swz(row)) + 8 * (c4 & 1)); }

DEV unsigned rtc() { return (unsigned)__builtin_amdgcn_s_memrealtime(); }
DEV void wait_rtc(unsigned t) { while ((int)(rtc() - t) < 0) {} }
DEV void hold_back(unsigned t0, int ticks) { const unsigned t = t0 + ticks; while ((int)(rtc() - t) < 0) __builtin_amdgcn_s_sleep(8); }
#define ASTAMP(i) do {} while (0)
#define STAMP(slot) do {} while (0)
#define WSTAMP(i) do {} while (0)

DEV bool spin_fail(unsigned& spins, rsrc_t rs, unsigned code) {
  ++spins;
  if (spins > SPIN_CAP) { st4(code, rs, OFF_FLAG + FL_ABORT * 128); return true; }
  if ((spins & 255u) == 0u) return __builtin_amdgcn_readfirstlane(ld4(rs, OFF_FLAG + FL_ABORT * 128)) != 0u;
  return false;
}
DEV void warm_weights(const Params& p, int slice) {
  const unsigned short* mats[4] = {p.wq, p.wk, p.wo, p.wv};
#pragma unroll 1
  for (int m = 0; m < 4; ++m) {
    const GAS v4u* src = (const GAS v4u*)mats[m] + slice * 4096 + threadIdx.x;
    v4u t[16];
#pragma unroll
    for (int i = 0; i < 16; ++i) t[i] = __builtin_nontemporal_load(src + i * 256);
#pragma unroll
    for (int i = 0; i < 16; ++i) asm volatile("" ::"v"(t[i]));
  }
}
DEV int near_half(const Params& p, int page, int lane) {
  const unsigned t = lane < NPROBE * 2 ? *(const GAS unsigned*)((GAS unsigned char*)p.ws + OFF_PAR + ((lane >> 1) * 4 + page * 2 + (lane & 1)) * 4) : ~0u;
  unsigned m = t;
#pragma unroll
  for (int d = 2; d < 64; d <<= 1) { const unsigned o = (unsigned)__shfl_xor((int)m, d); m = o < m ? o : m; }
  const unsigned m0 = __builtin_amdgcn_readlane(m, 0), m1 = __builtin_amdgcn_readlane(m, 1);
  return m1 < m0 ? 1 : 0;
}
DEV int qkv_block(int s, int h) { return h * 524288 + s * 8192; }
DEV unsigned wave_min(unsigned v) {
  const unsigned a = __builtin_amdgcn_readlane(v, 0), b = __builtin_amdgcn_readlane(v, 1), c = __builtin_amdgcn_readlane(v, 2);
  const unsigned m = a < b ? a : b;
  return m < c ? m : c;
}
DEV unsigned peek_flags(rsrc_t rs, int foff) { asm volatile("" ::: "memory"); return wave_min(ld4(rs, foff)); }
template <int SLEEP = 2> DEV unsigned wait_flags(rsrc_t rs, int foff, unsigned need, unsigned code) {
  unsigned spins = 0, v = 0;
  for (;;) {
    asm volatile("" ::: "memory");
    v = ld4(rs, foff);
    if (__all(v >= need)) break;
    if (spin_fail(spins, rs, code)) break;
    __builtin_amdgcn_s_sleep(SLEEP);
  }
  return wave_min(v);
}
DEV void wg_wait(rsrc_t rs, int w, int foff, unsigned need, unsigned code) {
  if (w == 0) wait_flags(rs, foff, need, code);
  BAR();
}

DEV float wg_rowsum(float v, LAS float* red, int w, int lane) {
  v = sum32(sum16(v));
  __syncthreads();
  if (lane < 16) red[w * 16 + lane] = v;
  __syncthreads();
  const int r = lane & 15;
  return (red[r] + red[16 + r]) + (red[32 + r] + red[48 + r]);
}
DEV void layernorm32(float (&f)[32], LAS float* red, int w, int lane) {
  float s = 0.f;
#pragma unroll
  for (int i = 0; i < 32; ++i) s += f[i];
  const float mean = wg_rowsum(s, red, w, lane) * (1.f / 512.f);
  float q = 0.f;
#pragma unroll
  for (int i = 0; i < 32; ++i) { const float d = f[i] - mean; q += d * d; }
  const float var = wg_rowsum(q, red, w, lane) * (1.f / 512.f);
  const float mb = rbf(mean), den = rbf(sqrtf(rbf(rbf(var) + 1e-5f)));
  const float inv = 1.f / den;
#pragma unroll
  for (int i = 0; i < 32; ++i) f[i] = rbf(rbf(f[i] - mb) * inv);
}

__global__ void __launch_bounds__(256) k_prep(Params p) {
  __shared__ float red[64];
  const int b = blockIdx.x, tid = threadIdx.x, lane = tid & 63, w = tid >> 6;
  GAS unsigned char* ws = (GAS unsigned char*)p.ws;
  unsigned pf;
  {
    unsigned lo, hi;
    asm volatile("s_getpc_b64 s[40:41]\n\ts_add_u32 s40, s40, _ZN12_GLOBAL__N_16k_mainENS_6ParamsE@rel32@lo+4\n\t"
                 "s_addc_u32 s41, s41, _ZN12_GLOBAL__N_16k_mainENS_6ParamsE@rel32@hi+12\n\ts_mov_b32 %0, s40\n\ts_mov_b32 %1, s41"
                 : "=s"(lo), "=s"(hi)::"s40", "s41", "scc");
    const unsigned long long a = ((unsigned long long)hi << 32) | lo;
    const int piece = (b >> 3) * 4 + w;
    pf = piece < CODE_KIB ? *(const GAS unsigned*)(a + piece * 1024 + lane * 16) : 0u;
  }
  WSTAMP(126);
  if (b < 128) {
    const int r = lane & 15, g = lane >> 4;
    float f[32];
#pragma unroll
    for (int i = 0; i < 4; ++i) {
      const v4u v = *(const GAS v4u*)((const GAS unsigned short*)p.x + (size_t)(16 * b + r) * 512 + 32 * (4 * w + i) + 8 * g);
#pragma unroll
      for (int j = 0; j < 4; ++j) { f[8 * i + 2 * j] = lo16(v[j]); f[8 * i + 2 * j + 1] = hi16(v[j]); }
    }
#pragma unroll
    for (int pass = 0; pass < 2; ++pass) {
      layernorm32(f, (LAS float*)red, w, lane);
#pragma unroll
      for (int i = 0; i < 4; ++i) {
        v4u o;
#pragma unroll
        for (int j = 0; j < 4; ++j) o[j] = pk2(f[8 * i + 2 * j], f[8 * i + 2 * j + 1]);
        *(GAS v4u*)(ws + (pass ? OFF_Y : OFF_XN) + (b * 16 + 4 * w + i) * 1024 + lane * 16) = o;
      }
    }
    asm volatile("" ::"v"(pf));
    WSTAMP(127);
    return;
  }
  asm volatile("" ::"v"(pf));
  const int c = b - 128;
  const unsigned tk0 = rtc();
  const v4u mk = {MARK, MARK, MARK, MARK};
  if ((c & 7) < 4 && c < 8 * NPROBE / 4) {
    const rsrc_t rs = make_rsrc(p.ws, WS_END);
    const int pb = (c >> 3) * 4 + (c & 3);
    const int base = (w >> 1) * 2 * MB + (72 + 3 * pb) * 16384 + (w & 1) * 8192;
    unsigned best = ~0u;
#pragma unroll 1
    for (int k = 0; k < 3; ++k) {
      if (lane < 8) st16(mk, rs, base + k * 16384 + lane * 16);
      WAIT_VM(0);
      const unsigned long long t0 = __builtin_amdgcn_s_memtime();
      unsigned v = ld4(rs, base + k * 16384);
      asm volatile("s_waitcnt vmcnt(0)" : "+v"(v)::"memory");
      const unsigned dt = (unsigned)(__builtin_amdgcn_s_memtime() - t0);
      best = dt < best ? dt : best;
    }
    if (lane == 0) *(GAS unsigned*)(ws + OFF_PAR + (pb * 4 + w) * 4) = best;
    WSTAMP(127);
    return;
  }
  const int cc = c < 32 ? (c >> 3) * 4 + (c & 3) : c - 16;
  if (T_PROBE && c != 0) hold_back(tk0, T_PROBE);
  for (int i = 1024 + cc * 256 + tid; i < (65 * 16384) / 16; i += 112 * 256) *(GAS v4u*)(ws + OFF_G + i * 16) = mk;
  if (cc == 2) {
    for (int i = tid; i < 512; i += 256) {
      const int kk = i >> 6, l = i & 63;
      const GAS unsigned short* m0 = (const GAS unsigned short*)p.mem0 + (l & 7) * 512 + 64 * kk + 16 * (l >> 4) + 2 * ((l >> 3) & 1);
      v4u o;
#pragma unroll
      for (int j = 0; j < 4; ++j) {
        const int e0 = 2 * j, e1 = 2 * j + 1;
        o[j] = nomark((unsigned)m0[4 * (e0 & 3) + (e0 >> 2)] | ((unsigned)m0[4 * (e1 & 3) + (e1 >> 2)] << 16));
      }
      *(GAS v4u*)(ws + OFF_G + i * 16) = o;
      *(GAS v4u*)(ws + OFF_G + 8192 + i * 16) = o;
    }
  }
  for (int i = cc * 256 + tid; i < MB / 16; i += 112 * 256) *(GAS v4u*)(ws + OFF_R + i * 16) = mk;
  if (cc == 1) for (int i = tid; i < FL_END * 128 / 16; i += 256) *(GAS v4u*)(ws + OFF_FLAG + i * 16) = v4u{0u, 0u, 0u, 0u};
  if (cc == 3) for (int i = tid; i < 1024; i += 256) *(GAS v4u*)(ws + OFF_KMF + i * 16) = v4u{0u, 0u, 0u, 0u};
  WSTAMP(127);
}

#define CHAIN_ASM \
  "s_getpc_b64 s[78:79]\n" \
  "pc0_%=:\n" \
  "s_add_u32 s78, s78, atab_%=-pc0_%=\n" \
  "s_addc_u32 s79, s79, 0\n" \
  "v_lshrrev_b32 v116, 2, %[l16]\n" \
  "buffer_load_dword v114, v116, %[rs], %[paro] offen\n" \
  "s_mov_b64 s[80:81], %[wqp]\n" \
  "s_add_u32 s80, s80, 0\n" \
  "s_addc_u32 s81, s81, 0\n" \
  "s_add_u32 s82, %[lbw], 0\n" \
  "s_mov_b32 m0, s82\n" \
  "s_add_u32 s82, s82, 1024\n" \
  "global_load_lds_dwordx4 v240, s[80:81]\n" \
  "s_mov_b32 m0, s82\n" \
  "s_add_u32 s82, s82, 1024\n" \
  "global_load_lds_dwordx4 v241, s[80:81]\n" \
  "s_mov_b32 m0, s82\n" \
  "s_add_u32 s82, s82, 1024\n" \
  "global_load_lds_dwordx4 v242, s[80:81]\n" \
  "s_mov_b32 m0, s82\n" \
  "s_add_u32 s82, s82, 1024\n" \
  "global_load_lds_dwordx4 v243, s[80:81]\n" \
  "s_mov_b64 s[80:81], %[wqp]\n" \
  "s_add_u32 s80, s80, 524288\n" \
  "s_addc_u32 s81, s81, 0\n" \
  "s_add_u32 s82, %[lbw], 16384\n" \
  "s_mov_b32 m0, s82\n" \
  "s_add_u32 s82, s82, 1024\n" \
  "global_load_lds_dwordx4 v240, s[80:81]\n" \
  "s_mov_b32 m0, s82\n" \
  "s_add_u32 s82, s82, 1024\n" \
  "global_load_lds_dwordx4 v241, s[80:81]\n" \
  "s_mov_b32 m0, s82\n" \
  "s_add_u32 s82, s82, 1024\n" \
  "global_load_lds_dwordx4 v242, s[80:81]\n" \
  "s_mov_b32 m0, s82\n" \
  "s_add_u32 s82, s82, 1024\n" \
  "global_load_lds_dwordx4 v243, s[80:81]\n" \
  "s_mov_b64 s[80:81], %[wqp]\n" \
  "s_add_u32 s80, s80, 1048576\n" \
  "s_addc_u32 s81, s81, 0\n" \
  "s_add_u32 s82, %[lbw], 32768\n" \
  "s_mov_b32 m0, s82\n" \
  "s_add_u32 s82, s82, 1024\n" \
  "global_load_lds_dwordx4 v240, s[80:81]\n" \
  "s_mov_b32 m0, s82\n" \
  "s_add_u32 s82, s82, 1024\n" \
  "global_load_lds_dwordx4 v241, s[80:81]\n" \
  "s_mov_b32 m0, s82\n" \
  "s_add_u32 s82, s82, 1024\n" \
  "global_load_lds_dwordx4 v242, s[80:81]\n" \
  "s_mov_b32 m0, s82\n" \
  "s_add_u32 s82, s82, 1024\n" \
  "global_load_lds_dwordx4 v243, s[80:81]\n" \
  "s_waitcnt vmcnt(8)\n" \
  "s_waitcnt lgkmcnt(0)\n" \
  "s_barrier\n" \
  "s_nop 1\n" \
  "v_min_u32_dpp v114, v114, v114 row_ror:4 row_mask:0xf bank_mask:0xf\n" \
  "s_nop 1\n" \
  "v_min_u32_dpp v114, v114, v114 row_ror:8 row_mask:0xf bank_mask:0xf\n" \
  "v_mov_b32 v115, v114\n" \
  "s_nop 1\n" \
  "v_permlane16_swap_b32 v115, v114\n" \
  "v_min_u32 v114, v114, v115\n" \
  "v_mov_b32 v115, v114\n" \
  "s_nop 1\n" \
  "v_permlane32_swap_b32 v115, v114\n" \
  "v_min_u32 v114, v114, v115\n" \
  "s_nop 1\n" \
  "v_readlane_b32 s88, v114, 0\n" \
  "v_readlane_b32 s89, v114, 1\n" \
  "v_readlane_b32 s90, v114, 2\n" \
  "v_readlane_b32 s91, v114, 3\n" \
  "s_nop 3\n" \
  "s_cmp_lt_u32 s89, s88\n" \
  "s_cselect_b32 s60, 1, 0\n" \
  "s_cmp_lt_u32 s91, s90\n" \
  "s_cselect_b32 s61, 1, 0\n" \
  "s_mov_b64 s[80:81], %[wqp]\n" \
  "s_add_u32 s80, s80, 1572864\n" \
  "s_addc_u32 s81, s81, 0\n" \
  "s_add_u32 s82, %[lbw], 49152\n" \
  "s_mov_b32 m0, s82\n" \
  "s_add_u32 s82, s82, 1024\n" \
  "global_load_lds_dwordx4 v240, s[80:81]\n" \
  "s_mov_b32 m0, s82\n" \
  "s_add_u32 s82, s82, 1024\n" \
  "global_load_lds_dwordx4 v241, s[80:81]\n" \
  "s_mov_b32 m0, s82\n" \
  "s_add_u32 s82, s82, 1024\n" \
  "global_load_lds_dwordx4 v242, s[80:81]\n" \
  "s_mov_b32 m0, s82\n" \
  "s_add_u32 s82, s82, 1024\n" \
  "global_load_lds_dwordx4 v243, s[80:81]\n" \
  "s_mov_b64 s[80:81], %[wqp]\n" \
  "s_add_u32 s80, s80, 2097152\n" \
  "s_addc_u32 s81, s81, 0\n" \
  "s_add_u32 s82, %[lbw], 65536\n" \
  "s_mov_b32 m0, s82\n" \
  "s_add_u32 s82, s82, 1024\n" \
  "global_load_lds_dwordx4 v240, s[80:81]\n" \
  "s_mov_b32 m0, s82\n" \
  "s_add_u32 s82, s82, 1024\n" \
  "global_load_lds_dwordx4 v241, s[80:81]\n" \
  "s_mov_b32 m0, s82\n" \
  "s_add_u32 s82, s82, 1024\n" \
  "global_load_lds_dwordx4 v242, s[80:81]\n" \
  "s_mov_b32 m0, s82\n" \
  "s_add_u32 s82, s82, 1024\n" \
  "global_load_lds_dwordx4 v243, s[80:81]\n" \
  "s_lshr_b32 s84, %[sig], 0\n" \
  "s_and_b32 s84, s84, 7\n" \
  "s_mul_i32 s84, s84, 72\n" \
  "s_add_u32 s84, s84, 0\n" \
  "s_add_u32 s88, s78, s84\n" \
  "s_addc_u32 s89, s79, 0\n" \
  "v_add_u32 v110, 0, v248\n" \
  "v_add_u32 v111, 0, v249\n" \
  "s_swappc_b64 s[86:87], s[88:89]\n" \
  "s_waitcnt vmcnt(12)\n" \
  "s_waitcnt lgkmcnt(0)\n" \
  "s_barrier\n" \
  "s_mov_b64 s[80:81], %[wqp]\n" \
  "s_add_u32 s80, s80, 2621440\n" \
  "s_addc_u32 s81, s81, 0\n" \
  "s_add_u32 s82, %[lbw], 81920\n" \
  "s_mov_b32 m0, s82\n" \
  "s_add_u32 s82, s82, 1024\n" \
  "global_load_lds_dwordx4 v240, s[80:81]\n" \
  "s_mov_b32 m0, s82\n" \
  "s_add_u32 s82, s82, 1024\n" \
  "global_load_lds_dwordx4 v241, s[80:81]\n" \
  "s_mov_b32 m0, s82\n" \
  "s_add_u32 s82, s82, 1024\n" \
  "global_load_lds_dwordx4 v242, s[80:81]\n" \
  "s_mov_b32 m0, s82\n" \
  "s_add_u32 s82, s82, 1024\n" \
  "global_load_lds_dwordx4 v243, s[80:81]\n" \
  "s_mov_b64 s[80:81], %[wqp]\n" \
  "s_add_u32 s80, s80, 3145728\n" \
  "s_addc_u32 s81, s81, 0\n" \
  "s_add_u32 s82, %[lbw], 98304\n" \
  "s_mov_b32 m0, s82\n" \
  "s_add_u32 s82, s82, 1024\n" \
  "global_load_lds_dwordx4 v240, s[80:81]\n" \
  "s_mov_b32 m0, s82\n" \
  "s_add_u32 s82, s82, 1024\n" \
  "global_load_lds_dwordx4 v241, s[80:81]\n" \
  "s_mov_b32 m0, s82\n" \
  "s_add_u32 s82, s82, 1024\n" \
  "global_load_lds_dwordx4 v242, s[80:81]\n" \
  "s_mov_b32 m0, s82\n" \
  "s_add_u32 s82, s82, 1024\n" \
  "global_load_lds_dwordx4 v243, s[80:81]\n" \
  "s_lshr_b32 s84, %[sig], 3\n" \
  "s_and_b32 s84, s84, 7\n" \
  "s_mul_i32 s84, s84, 72\n" \
  "s_add_u32 s84, s84, 0\n" \
  "s_add_u32 s88, s78, s84\n" \
  "s_addc_u32 s89, s79, 0\n" \
  "v_add_u32 v110, 16384, v248\n" \
  "v_add_u32 v111, 16384, v249\n" \
  "s_swappc_b64 s[86:87], s[88:89]\n" \
  "s_waitcnt vmcnt(16)\n" \
  "s_waitcnt lgkmcnt(0)\n" \
  "s_barrier\n" \
  "s_mov_b64 s[80:81], %[wqp]\n" \
  "s_add_u32 s80, s80, 3670016\n" \
  "s_addc_u32 s81, s81, 0\n" \
  "s_add_u32 s82, %[lbw], 114688\n" \
  "s_mov_b32 m0, s82\n" \
  "s_add_u32 s82, s82, 1024\n" \
  "global_load_lds_dwordx4 v240, s[80:81]\n" \
  "s_mov_b32 m0, s82\n" \
  "s_add_u32 s82, s82, 1024\n" \
  "global_load_lds_dwordx4 v241, s[80:81]\n" \
  "s_mov_b32 m0, s82\n" \
  "s_add_u32 s82, s82, 1024\n" \
  "global_load_lds_dwordx4 v242, s[80:81]\n" \
  "s_mov_b32 m0, s82\n" \
  "s_add_u32 s82, s82, 1024\n" \
  "global_load_lds_dwordx4 v243, s[80:81]\n" \
  "s_mov_b64 s[80:81], %[wkp]\n" \
  "s_add_u32 s80, s80, 0\n" \
  "s_addc_u32 s81, s81, 0\n" \
  "s_add_u32 s82, %[lbw], 131072\n" \
  "s_mov_b32 m0, s82\n" \
  "s_add_u32 s82, s82, 1024\n" \
  "global_load_lds_dwordx4 v240, s[80:81]\n" \
  "s_mov_b32 m0, s82\n" \
  "s_add_u32 s82, s82, 1024\n" \
  "global_load_lds_dwordx4 v241, s[80:81]\n" \
  "s_mov_b32 m0, s82\n" \
  "s_add_u32 s82, s82, 1024\n" \
  "global_load_lds_dwordx4 v242, s[80:81]\n" \
  "s_mov_b32 m0, s82\n" \
  "s_add_u32 s82, s82, 1024\n" \
  "global_load_lds_dwordx4 v243, s[80:81]\n" \
  "s_lshr_b32 s84, %[sig], 6\n" \
  "s_and_b32 s84, s84, 7\n" \
  "s_mul_i32 s84, s84, 72\n" \
  "s_add_u32 s84, s84, 0\n" \
  "s_add_u32 s88, s78, s84\n" \
  "s_addc_u32 s89, s79, 0\n" \
  "v_add_u32 v110, 32768, v248\n" \
  "v_add_u32 v111, 32768, v249\n" \
  "s_swappc_b64 s[86:87], s[88:89]\n" \
  "s_waitcnt vmcnt(20)\n" \
  "s_waitcnt lgkmcnt(0)\n" \
  "s_barrier\n" \
  "s_mov_b64 s[80:81], %[wkp]\n" \
  "s_add_u32 s80, s80, 524288\n" \
  "s_addc_u32 s81, s81, 0\n" \
  "s_add_u32 s82, %[lbw], 147456\n" \
  "s_mov_b32 m0, s82\n" \
  "s_add_u32 s82, s82, 1024\n" \
  "global_load_lds_dwordx4 v240, s[80:81]\n" \
  "s_mov_b32 m0, s82\n" \
  "s_add_u32 s82, s82, 1024\n" \
  "global_load_lds_dwordx4 v241, s[80:81]\n" \
  "s_mov_b32 m0, s82\n" \
  "s_add_u32 s82, s82, 1024\n" \
  "global_load_lds_dwordx4 v242, s[80:81]\n" \
  "s_mov_b32 m0, s82\n" \
  "s_add_u32 s82, s82, 1024\n" \
  "global_load_lds_dwordx4 v243, s[80:81]\n" \
  "s_mov_b64 s[80:81], %[wkp]\n" \
  "s_add_u32 s80, s80, 1048576\n" \
  "s_addc_u32 s81, s81, 0\n" \
  "s_add_u32 s82, %[lbw], 0\n" \
  "s_mov_b32 m0, s82\n" \
  "s_add_u32 s82, s82, 1024\n" \
  "global_load_lds_dwordx4 v240, s[80:81]\n" \
  "s_mov_b32 m0, s82\n" \
  "s_add_u32 s82, s82, 1024\n" \
  "global_load_lds_dwordx4 v241, s[80:81]\n" \
  "s_mov_b32 m0, s82\n" \
  "s_add_u32 s82, s82, 1024\n" \
  "global_load_lds_dwordx4 v242, s[80:81]\n" \
  "s_mov_b32 m0, s82\n" \
  "s_add_u32 s82, s82, 1024\n" \
  "global_load_lds_dwordx4 v243, s[80:81]\n" \
  "s_lshr_b32 s84, %[sig], 9\n" \
  "s_and_b32 s84, s84, 7\n" \
  "s_mul_i32 s84, s84, 72\n" \
  "s_add_u32 s84, s84, 0\n" \
  "s_add_u32 s88, s78, s84\n" \
  "s_addc_u32 s89, s79, 0\n" \
  "v_add_u32 v110, 49152, v248\n" \
  "v_add_u32 v111, 49152, v249\n" \
  "s_swappc_b64 s[86:87], s[88:89]\n" \
  "s_waitcnt vmcnt(24)\n" \
  "s_waitcnt lgkmcnt(0)\n" \
  "s_barrier\n" \
  "s_mov_b64 s[80:81], %[wkp]\n" \
  "s_add_u32 s80, s80, 1572864\n" \
  "s_addc_u32 s81, s81, 0\n" \
  "s_add_u32 s82, %[lbw], 16384\n" \
  "s_mov_b32 m0, s82\n" \
  "s_add_u32 s82, s82, 1024\n" \
  "global_load_lds_dwordx4 v240, s[80:81]\n" \
  "s_mov_b32 m0, s82\n" \
  "s_add_u32 s82, s82, 1024\n" \
  "global_load_lds_dwordx4 v241, s[80:81]\n" \
  "s_mov_b32 m0, s82\n" \
  "s_add_u32 s82, s82, 1024\n" \
  "global_load_lds_dwordx4 v242, s[80:81]\n" \
  "s_mov_b32 m0, s82\n" \
  "s_add_u32 s82, s82, 1024\n" \
  "global_load_lds_dwordx4 v243, s[80:81]\n" \
  "s_mov_b64 s[80:81], %[wkp]\n" \
  "s_add_u32 s80, s80, 2097152\n" \
  "s_addc_u32 s81, s81, 0\n" \
  "s_add_u32 s82, %[lbw], 32768\n" \
  "s_mov_b32 m0, s82\n" \
  "s_add_u32 s82, s82, 1024\n" \
  "global_load_lds_dwordx4 v240, s[80:81]\n" \
  "s_mov_b32 m0, s82\n" \
  "s_add_u32 s82, s82, 1024\n" \
  "global_load_lds_dwordx4 v241, s[80:81]\n" \
  "s_mov_b32 m0, s82\n" \
  "s_add_u32 s82, s82, 1024\n" \
  "global_load_lds_dwordx4 v242, s[80:81]\n" \
  "s_mov_b32 m0, s82\n" \
  "s_add_u32 s82, s82, 1024\n" \
  "global_load_lds_dwordx4 v243, s[80:81]\n" \
  "s_lshr_b32 s84, %[sig], 12\n" \
  "s_and_b32 s84, s84, 7\n" \
  "s_mul_i32 s84, s84, 72\n" \
  "s_add_u32 s84, s84, 0\n" \
  "s_add_u32 s88, s78, s84\n" \
  "s_addc_u32 s89, s79, 0\n" \
  "v_add_u32 v110, 65536, v248\n" \
  "v_add_u32 v111, 65536, v249\n" \
  "s_swappc_b64 s[86:87], s[88:89]\n" \
  "s_waitcnt vmcnt(28)\n" \
  "s_waitcnt lgkmcnt(0)\n" \
  "s_barrier\n" \
  "s_mov_b64 s[80:81], %[wkp]\n" \
  "s_add_u32 s80, s80, 2621440\n" \
  "s_addc_u32 s81, s81, 0\n" \
  "s_add_u32 s82, %[lbw], 49152\n" \
  "s_mov_b32 m0, s82\n" \
  "s_add_u32 s82, s82, 1024\n" \
  "global_load_lds_dwordx4 v240, s[80:81]\n" \
  "s_mov_b32 m0, s82\n" \
  "s_add_u32 s82, s82, 1024\n" \
  "global_load_lds_dwordx4 v241, s[80:81]\n" \
  "s_mov_b32 m0, s82\n" \
  "s_add_u32 s82, s82, 1024\n" \
  "global_load_lds_dwordx4 v242, s[80:81]\n" \
  "s_mov_b32 m0, s82\n" \
  "s_add_u32 s82, s82, 1024\n" \
  "global_load_lds_dwordx4 v243, s[80:81]\n" \
  "s_mov_b64 s[80:81], %[wkp]\n" \
  "s_add_u32 s80, s80, 3145728\n" \
  "s_addc_u32 s81, s81, 0\n" \
  "s_add_u32 s82, %[lbw], 65536\n" \
  "s_mov_b32 m0, s82\n" \
  "s_add_u32 s82, s82, 1024\n" \
  "global_load_lds_dwordx4 v240, s[80:81]\n" \
  "s_mov_b32 m0, s82\n" \
  "s_add_u32 s82, s82, 1024\n" \
  "global_load_lds_dwordx4 v241, s[80:81]\n" \
  "s_mov_b32 m0, s82\n" \
  "s_add_u32 s82, s82, 1024\n" \
  "global_load_lds_dwordx4 v242, s[80:81]\n" \
  "s_mov_b32 m0, s82\n" \
  "s_add_u32 s82, s82, 1024\n" \
  "global_load_lds_dwordx4 v243, s[80:81]\n" \
  "s_lshr_b32 s84, %[sig], 15\n" \
  "s_and_b32 s84, s84, 7\n" \
  "s_mul_i32 s84, s84, 72\n" \
  "s_add_u32 s84, s84, 0\n" \
  "s_add_u32 s88, s78, s84\n" \
  "s_addc_u32 s89, s79, 0\n" \
  "v_add_u32 v110, 81920, v248\n" \
  "v_add_u32 v111, 81920, v249\n" \
  "s_swappc_b64 s[86:87], s[88:89]\n" \
  "s_waitcnt vmcnt(32)\n" \
  "s_waitcnt lgkmcnt(0)\n" \
  "s_barrier\n" \
  "s_mov_b64 s[80:81], %[wkp]\n" \
  "s_add_u32 s80, s80, 3670016\n" \
  "s_addc_u32 s81, s81, 0\n" \
  "s_add_u32 s82, %[lbw], 81920\n" \
  "s_mov_b32 m0, s82\n" \
  "s_add_u32 s82, s82, 1024\n" \
  "global_load_lds_dwordx4 v240, s[80:81]\n" \
  "s_mov_b32 m0, s82\n" \
  "s_add_u32 s82, s82, 1024\n" \
  "global_load_lds_dwordx4 v241, s[80:81]\n" \
  "s_mov_b32 m0, s82\n" \
  "s_add_u32 s82, s82, 1024\n" \
  "global_load_lds_dwordx4 v242, s[80:81]\n" \
  "s_mov_b32 m0, s82\n" \
  "s_add_u32 s82, s82, 1024\n" \
  "global_load_lds_dwordx4 v243, s[80:81]\n" \
  "s_lshr_b32 s84, %[sig], 18\n" \
  "s_and_b32 s84, s84, 7\n" \
  "s_mul_i32 s84, s84, 72\n" \
  "s_add_u32 s84, s84, 0\n" \
  "s_add_u32 s88, s78, s84\n" \
  "s_addc_u32 s89, s79, 0\n" \
  "v_add_u32 v110, 98304, v248\n" \
  "v_add_u32 v111, 98304, v249\n" \
  "s_swappc_b64 s[86:87], s[88:89]\n" \
  "s_waitcnt vmcnt(32)\n" \
  "s_waitcnt lgkmcnt(0)\n" \
  "s_barrier\n" \
  "s_mov_b64 s[80:81], %[wop]\n" \
  "s_add_u32 s80, s80, 0\n" \
  "s_addc_u32 s81, s81, 0\n" \
  "s_add_u32 s82, %[lbw], 98304\n" \
  "s_mov_b32 m0, s82\n" \
  "s_add_u32 s82, s82, 1024\n" \
  "global_load_lds_dwordx4 v244, s[80:81]\n" \
  "s_mov_b32 m0, s82\n" \
  "s_add_u32 s82, s82, 1024\n" \
  "global_load_lds_dwordx4 v245, s[80:81]\n" \
  "s_mov_b32 m0, s82\n" \
  "s_add_u32 s82, s82, 1024\n" \
  "global_load_lds_dwordx4 v246, s[80:81]\n" \
  "s_mov_b32 m0, s82\n" \
  "s_add_u32 s82, s82, 1024\n" \
  "global_load_lds_dwordx4 v247, s[80:81]\n" \
  "s_lshr_b32 s84, %[sig], 21\n" \
  "s_and_b32 s84, s84, 7\n" \
  "s_mul_i32 s84, s84, 72\n" \
  "s_add_u32 s84, s84, 0\n" \
  "s_add_u32 s88, s78, s84\n" \
  "s_addc_u32 s89, s79, 0\n" \
  "v_add_u32 v110, 114688, v248\n" \
  "v_add_u32 v111, 114688, v249\n" \
  "s_swappc_b64 s[86:87], s[88:89]\n" \
  "s_waitcnt vmcnt(32)\n" \
  "s_waitcnt lgkmcnt(0)\n" \
  "s_barrier\n" \
  "s_mov_b64 s[80:81], %[wop]\n" \
  "s_add_u32 s80, s80, 256\n" \
  "s_addc_u32 s81, s81, 0\n" \
  "s_add_u32 s82, %[lbw], 114688\n" \
  "s_mov_b32 m0, s82\n" \
  "s_add_u32 s82, s82, 1024\n" \
  "global_load_lds_dwordx4 v244, s[80:81]\n" \
  "s_mov_b32 m0, s82\n" \
  "s_add_u32 s82, s82, 1024\n" \
  "global_load_lds_dwordx4 v245, s[80:81]\n" \
  "s_mov_b32 m0, s82\n" \
  "s_add_u32 s82, s82, 1024\n" \
  "global_load_lds_dwordx4 v246, s[80:81]\n" \
  "s_mov_b32 m0, s82\n" \
  "s_add_u32 s82, s82, 1024\n" \
  "global_load_lds_dwordx4 v247, s[80:81]\n" \
  "s_lshr_b32 s84, %[sig], 0\n" \
  "s_and_b32 s84, s84, 7\n" \
  "s_mul_i32 s84, s84, 72\n" \
  "s_add_u32 s84, s84, 576\n" \
  "s_add_u32 s88, s78, s84\n" \
  "s_addc_u32 s89, s79, 0\n" \
  "v_add_u32 v110, 131072, v248\n" \
  "v_add_u32 v111, 131072, v249\n" \
  "s_swappc_b64 s[86:87], s[88:89]\n" \
  "s_waitcnt vmcnt(32)\n" \
  "s_waitcnt lgkmcnt(0)\n" \
  "s_barrier\n" \
  "s_mov_b64 s[80:81], %[wop]\n" \
  "s_add_u32 s80, s80, 65536\n" \
  "s_addc_u32 s81, s81, 0\n" \
  "s_add_u32 s82, %[lbw], 131072\n" \
  "s_mov_b32 m0, s82\n" \
  "s_add_u32 s82, s82, 1024\n" \
  "global_load_lds_dwordx4 v244, s[80:81]\n" \
  "s_mov_b32 m0, s82\n" \
  "s_add_u32 s82, s82, 1024\n" \
  "global_load_lds_dwordx4 v245, s[80:81]\n" \
  "s_mov_b32 m0, s82\n" \
  "s_add_u32 s82, s82, 1024\n" \
  "global_load_lds_dwordx4 v246, s[80:81]\n" \
  "s_mov_b32 m0, s82\n" \
  "s_add_u32 s82, s82, 1024\n" \
  "global_load_lds_dwordx4 v247, s[80:81]\n" \
  "s_lshr_b32 s84, %[sig], 3\n" \
  "s_and_b32 s84, s84, 7\n" \
  "s_mul_i32 s84, s84, 72\n" \
  "s_add_u32 s84, s84, 576\n" \
  "s_add_u32 s88, s78, s84\n" \
  "s_addc_u32 s89, s79, 0\n" \
  "v_add_u32 v110, 147456, v248\n" \
  "v_add_u32 v111, 147456, v249\n" \
  "s_swappc_b64 s[86:87], s[88:89]\n" \
  "s_waitcnt vmcnt(32)\n" \
  "s_waitcnt lgkmcnt(0)\n" \
  "s_barrier\n" \
  "s_mov_b64 s[80:81], %[wop]\n" \
  "s_add_u32 s80, s80, 65792\n" \
  "s_addc_u32 s81, s81, 0\n" \
  "s_add_u32 s82, %[lbw], 147456\n" \
  "s_mov_b32 m0, s82\n" \
  "s_add_u32 s82, s82, 1024\n" \
  "global_load_lds_dwordx4 v244, s[80:81]\n" \
  "s_mov_b32 m0, s82\n" \
  "s_add_u32 s82, s82, 1024\n" \
  "global_load_lds_dwordx4 v245, s[80:81]\n" \
  "s_mov_b32 m0, s82\n" \
  "s_add_u32 s82, s82, 1024\n" \
  "global_load_lds_dwordx4 v246, s[80:81]\n" \
  "s_mov_b32 m0, s82\n" \
  "s_add_u32 s82, s82, 1024\n" \
  "global_load_lds_dwordx4 v247, s[80:81]\n" \
  "s_lshr_b32 s84, %[sig], 6\n" \
  "s_and_b32 s84, s84, 7\n" \
  "s_mul_i32 s84, s84, 72\n" \
  "s_add_u32 s84, s84, 576\n" \
  "s_add_u32 s88, s78, s84\n" \
  "s_addc_u32 s89, s79, 0\n" \
  "v_add_u32 v110, 0, v248\n" \
  "v_add_u32 v111, 0, v249\n" \
  "s_swappc_b64 s[86:87], s[88:89]\n" \
  "s_waitcnt vmcnt(32)\n" \
  "s_waitcnt lgkmcnt(0)\n" \
  "s_barrier\n" \
  "s_mov_b64 s[80:81], %[wvp]\n" \
  "s_add_u32 s80, s80, 0\n" \
  "s_addc_u32 s81, s81, 0\n" \
  "s_add_u32 s82, %[lbw], 0\n" \
  "s_mov_b32 m0, s82\n" \
  "s_add_u32 s82, s82, 1024\n" \
  "global_load_lds_dwordx4 v240, s[80:81]\n" \
  "s_mov_b32 m0, s82\n" \
  "s_add_u32 s82, s82, 1024\n" \
  "global_load_lds_dwordx4 v241, s[80:81]\n" \
  "s_mov_b32 m0, s82\n" \
  "s_add_u32 s82, s82, 1024\n" \
  "global_load_lds_dwordx4 v242, s[80:81]\n" \
  "s_mov_b32 m0, s82\n" \
  "s_add_u32 s82, s82, 1024\n" \
  "global_load_lds_dwordx4 v243, s[80:81]\n" \
  "s_lshr_b32 s84, %[sig], 9\n" \
  "s_and_b32 s84, s84, 7\n" \
  "s_mul_i32 s84, s84, 72\n" \
  "s_add_u32 s84, s84, 576\n" \
  "s_add_u32 s88, s78, s84\n" \
  "s_addc_u32 s89, s79, 0\n" \
  "v_add_u32 v110, 16384, v248\n" \
  "v_add_u32 v111, 16384, v249\n" \
  "s_swappc_b64 s[86:87], s[88:89]\n" \
  "s_waitcnt vmcnt(32)\n" \
  "s_waitcnt lgkmcnt(0)\n" \
  "s_barrier\n" \
  "s_mov_b64 s[80:81], %[wvp]\n" \
  "s_add_u32 s80, s80, 524288\n" \
  "s_addc_u32 s81, s81, 0\n" \
  "s_add_u32 s82, %[lbw], 16384\n" \
  "s_mov_b32 m0, s82\n" \
  "s_add_u32 s82, s82, 1024\n" \
  "global_load_lds_dwordx4 v240, s[80:81]\n" \
  "s_mov_b32 m0, s82\n" \
  "s_add_u32 s82, s82, 1024\n" \
  "global_load_lds_dwordx4 v241, s[80:81]\n" \
  "s_mov_b32 m0, s82\n" \
  "s_add_u32 s82, s82, 1024\n" \
  "global_load_lds_dwordx4 v242, s[80:81]\n" \
  "s_mov_b32 m0, s82\n" \
  "s_add_u32 s82, s82, 1024\n" \
  "global_load_lds_dwordx4 v243, s[80:81]\n" \
  "s_lshr_b32 s84, %[sig], 12\n" \
  "s_and_b32 s84, s84, 7\n" \
  "s_mul_i32 s84, s84, 72\n" \
  "s_add_u32 s84, s84, 576\n" \
  "s_add_u32 s88, s78, s84\n" \
  "s_addc_u32 s89, s79, 0\n" \
  "v_add_u32 v110, 32768, v248\n" \
  "v_add_u32 v111, 32768, v249\n" \
  "s_swappc_b64 s[86:87], s[88:89]\n" \
  "s_waitcnt vmcnt(32)\n" \
  "s_waitcnt lgkmcnt(0)\n" \
  "s_barrier\n" \
  "s_mov_b64 s[80:81], %[wvp]\n" \
  "s_add_u32 s80, s80, 1048576\n" \
  "s_addc_u32 s81, s81, 0\n" \
  "s_add_u32 s82, %[lbw], 32768\n" \
  "s_mov_b32 m0, s82\n" \
  "s_add_u32 s82, s82, 1024\n" \
  "global_load_lds_dwordx4 v240, s[80:81]\n" \
  "s_mov_b32 m0, s82\n" \
  "s_add_u32 s82, s82, 1024\n" \
  "global_load_lds_dwordx4 v241, s[80:81]\n" \
  "s_mov_b32 m0, s82\n" \
  "s_add_u32 s82, s82, 1024\n" \
  "global_load_lds_dwordx4 v242, s[80:81]\n" \
  "s_mov_b32 m0, s82\n" \
  "s_add_u32 s82, s82, 1024\n" \
  "global_load_lds_dwordx4 v243, s[80:81]\n" \
  "s_lshr_b32 s84, %[sig], 15\n" \
  "s_and_b32 s84, s84, 7\n" \
  "s_mul_i32 s84, s84, 72\n" \
  "s_add_u32 s84, s84, 576\n" \
  "s_add_u32 s88, s78, s84\n" \
  "s_addc_u32 s89, s79, 0\n" \
  "v_add_u32 v110, 49152, v248\n" \
  "v_add_u32 v111, 49152, v249\n" \
  "s_swappc_b64 s[86:87], s[88:89]\n" \
  "s_waitcnt vmcnt(32)\n" \
  "s_waitcnt lgkmcnt(0)\n" \
  "s_barrier\n" \
  "s_mov_b64 s[80:81], %[wvp]\n" \
  "s_add_u32 s80, s80, 1572864\n" \
  "s_addc_u32 s81, s81, 0\n" \
  "s_add_u32 s82, %[lbw], 49152\n" \
  "s_mov_b32 m0, s82\n" \
  "s_add_u32 s82, s82, 1024\n" \
  "global_load_lds_dwordx4 v240, s[80:81]\n" \
  "s_mov_b32 m0, s82\n" \
  "s_add_u32 s82, s82, 1024\n" \
  "global_load_lds_dwordx4 v241, s[80:81]\n" \
  "s_mov_b32 m0, s82\n" \
  "s_add_u32 s82, s82, 1024\n" \
  "global_load_lds_dwordx4 v242, s[80:81]\n" \
  "s_mov_b32 m0, s82\n" \
  "s_add_u32 s82, s82, 1024\n" \
  "global_load_lds_dwordx4 v243, s[80:81]\n" \
  "s_lshr_b32 s84, %[sig], 18\n" \
  "s_and_b32 s84, s84, 7\n" \
  "s_mul_i32 s84, s84, 72\n" \
  "s_add_u32 s84, s84, 576\n" \
  "s_add_u32 s88, s78, s84\n" \
  "s_addc_u32 s89, s79, 0\n" \
  "v_add_u32 v110, 65536, v248\n" \
  "v_add_u32 v111, 65536, v249\n" \
  "s_swappc_b64 s[86:87], s[88:89]\n" \
  "s_waitcnt vmcnt(32)\n" \
  "s_waitcnt lgkmcnt(0)\n" \
  "s_barrier\n" \
  "s_mov_b64 s[80:81], %[wvp]\n" \
  "s_add_u32 s80, s80, 2097152\n" \
  "s_addc_u32 s81, s81, 0\n" \
  "s_add_u32 s82, %[lbw], 65536\n" \
  "s_mov_b32 m0, s82\n" \
  "s_add_u32 s82, s82, 1024\n" \
  "global_load_lds_dwordx4 v240, s[80:81]\n" \
  "s_mov_b32 m0, s82\n" \
  "s_add_u32 s82, s82, 1024\n" \
  "global_load_lds_dwordx4 v241, s[80:81]\n" \
  "s_mov_b32 m0, s82\n" \
  "s_add_u32 s82, s82, 1024\n" \
  "global_load_lds_dwordx4 v242, s[80:81]\n" \
  "s_mov_b32 m0, s82\n" \
  "s_add_u32 s82, s82, 1024\n" \
  "global_load_lds_dwordx4 v243, s[80:81]\n" \
  "s_lshr_b32 s84, %[sig], 21\n" \
  "s_and_b32 s84, s84, 7\n" \
  "s_mul_i32 s84, s84, 72\n" \
  "s_add_u32 s84, s84, 576\n" \
  "s_add_u32 s88, s78, s84\n" \
  "s_addc_u32 s89, s79, 0\n" \
  "v_add_u32 v110, 81920, v248\n" \
  "v_add_u32 v111, 81920, v249\n" \
  "s_swappc_b64 s[86:87], s[88:89]\n" \
  "s_waitcnt vmcnt(32)\n" \
  "s_waitcnt lgkmcnt(0)\n" \
  "s_barrier\n" \
  "s_mov_b64 s[80:81], %[wvp]\n" \
  "s_add_u32 s80, s80, 2621440\n" \
  "s_addc_u32 s81, s81, 0\n" \
  "s_add_u32 s82, %[lbw], 81920\n" \
  "s_mov_b32 m0, s82\n" \
  "s_add_u32 s82, s82, 1024\n" \
  "global_load_lds_dwordx4 v240, s[80:81]\n" \
  "s_mov_b32 m0, s82\n" \
  "s_add_u32 s82, s82, 1024\n" \
  "global_load_lds_dwordx4 v241, s[80:81]\n" \
  "s_mov_b32 m0, s82\n" \
  "s_add_u32 s82, s82, 1024\n" \
  "global_load_lds_dwordx4 v242, s[80:81]\n" \
  "s_mov_b32 m0, s82\n" \
  "s_add_u32 s82, s82, 1024\n" \
  "global_load_lds_dwordx4 v243, s[80:81]\n" \
  "s_lshr_b32 s84, %[role], 5\n" \
  "s_and_b32 s84, s84, 1\n" \
  "s_cmp_eq_u32 s84, 0\n" \
  "s_cbranch_scc0 nowo16_%=\n" \
  "v_add_u32 v110, 98304, v252\n" \
  "v_add_u32 v111, 98304, v253\n" \
  "v_add_u32 v112, 98304, v254\n" \
  "v_add_u32 v113, 98304, v255\n" \
  "ds_read_b64_tr_b16 v[0:1], v110 offset:0\n" \
  "ds_read_b64_tr_b16 v[2:3], v112 offset:512\n" \
  "ds_read_b64_tr_b16 v[4:5], v110 offset:8192\n" \
  "ds_read_b64_tr_b16 v[6:7], v112 offset:8704\n" \
  "ds_read_b64_tr_b16 v[16:17], v110 offset:8\n" \
  "ds_read_b64_tr_b16 v[18:19], v112 offset:520\n" \
  "ds_read_b64_tr_b16 v[20:21], v110 offset:8200\n" \
  "ds_read_b64_tr_b16 v[22:23], v112 offset:8712\n" \
  "ds_read_b64_tr_b16 v[32:33], v111 offset:0\n" \
  "ds_read_b64_tr_b16 v[34:35], v113 offset:512\n" \
  "ds_read_b64_tr_b16 v[36:37], v111 offset:8192\n" \
  "ds_read_b64_tr_b16 v[38:39], v113 offset:8704\n" \
  "ds_read_b64_tr_b16 v[48:49], v111 offset:8\n" \
  "ds_read_b64_tr_b16 v[50:51], v113 offset:520\n" \
  "ds_read_b64_tr_b16 v[52:53], v111 offset:8200\n" \
  "s_waitcnt lgkmcnt(8)\n" \
  "ds_read_b64_tr_b16 v[54:55], v113 offset:8712\n" \
  "nowo16_%=:\n" \
  "s_waitcnt vmcnt(32)\n" \
  "s_waitcnt lgkmcnt(0)\n" \
  "s_barrier\n" \
  "s_mov_b64 s[80:81], %[wvp]\n" \
  "s_add_u32 s80, s80, 3145728\n" \
  "s_addc_u32 s81, s81, 0\n" \
  "s_add_u32 s82, %[lbw], 98304\n" \
  "s_mov_b32 m0, s82\n" \
  "s_add_u32 s82, s82, 1024\n" \
  "global_load_lds_dwordx4 v240, s[80:81]\n" \
  "s_mov_b32 m0, s82\n" \
  "s_add_u32 s82, s82, 1024\n" \
  "global_load_lds_dwordx4 v241, s[80:81]\n" \
  "s_mov_b32 m0, s82\n" \
  "s_add_u32 s82, s82, 1024\n" \
  "global_load_lds_dwordx4 v242, s[80:81]\n" \
  "s_mov_b32 m0, s82\n" \
  "s_add_u32 s82, s82, 1024\n" \
  "global_load_lds_dwordx4 v243, s[80:81]\n" \
  "s_lshr_b32 s84, %[role], 5\n" \
  "s_and_b32 s84, s84, 1\n" \
  "s_cmp_eq_u32 s84, 1\n" \
  "s_cbranch_scc0 nowo17_%=\n" \
  "v_add_u32 v110, 114688, v252\n" \
  "v_add_u32 v111, 114688, v253\n" \
  "v_add_u32 v112, 114688, v254\n" \
  "v_add_u32 v113, 114688, v255\n" \
  "ds_read_b64_tr_b16 v[0:1], v110 offset:0\n" \
  "ds_read_b64_tr_b16 v[2:3], v112 offset:512\n" \
  "ds_read_b64_tr_b16 v[4:5], v110 offset:8192\n" \
  "ds_read_b64_tr_b16 v[6:7], v112 offset:8704\n" \
  "ds_read_b64_tr_b16 v[16:17], v110 offset:8\n" \
  "ds_read_b64_tr_b16 v[18:19], v112 offset:520\n" \
  "ds_read_b64_tr_b16 v[20:21], v110 offset:8200\n" \
  "ds_read_b64_tr_b16 v[22:23], v112 offset:8712\n" \
  "ds_read_b64_tr_b16 v[32:33], v111 offset:0\n" \
  "ds_read_b64_tr_b16 v[34:35], v113 offset:512\n" \
  "ds_read_b64_tr_b16 v[36:37], v111 offset:8192\n" \
  "ds_read_b64_tr_b16 v[38:39], v113 offset:8704\n" \
  "ds_read_b64_tr_b16 v[48:49], v111 offset:8\n" \
  "ds_read_b64_tr_b16 v[50:51], v113 offset:520\n" \
  "ds_read_b64_tr_b16 v[52:53], v111 offset:8200\n" \
  "s_waitcnt lgkmcnt(8)\n" \
  "ds_read_b64_tr_b16 v[54:55], v113 offset:8712\n" \
  "nowo17_%=:\n" \
  "s_waitcnt vmcnt(32)\n" \
  "s_waitcnt lgkmcnt(0)\n" \
  "s_barrier\n" \
  "s_mov_b64 s[80:81], %[wvp]\n" \
  "s_add_u32 s80, s80, 3670016\n" \
  "s_addc_u32 s81, s81, 0\n" \
  "s_add_u32 s82, %[lbw], 114688\n" \
  "s_mov_b32 m0, s82\n" \
  "s_add_u32 s82, s82, 1024\n" \
  "global_load_lds_dwordx4 v240, s[80:81]\n" \
  "s_mov_b32 m0, s82\n" \
  "s_add_u32 s82, s82, 1024\n" \
  "global_load_lds_dwordx4 v241, s[80:81]\n" \
  "s_mov_b32 m0, s82\n" \
  "s_add_u32 s82, s82, 1024\n" \
  "global_load_lds_dwordx4 v242, s[80:81]\n" \
  "s_mov_b32 m0, s82\n" \
  "s_add_u32 s82, s82, 1024\n" \
  "global_load_lds_dwordx4 v243, s[80:81]\n" \
  "s_lshr_b32 s84, %[role], 5\n" \
  "s_and_b32 s84, s84, 1\n" \
  "s_cmp_eq_u32 s84, 0\n" \
  "s_cbranch_scc0 nowo18_%=\n" \
  "v_add_u32 v110, 131072, v252\n" \
  "v_add_u32 v111, 131072, v253\n" \
  "v_add_u32 v112, 131072, v254\n" \
  "v_add_u32 v113, 131072, v255\n" \
  "ds_read_b64_tr_b16 v[8:9], v110 offset:0\n" \
  "ds_read_b64_tr_b16 v[10:11], v112 offset:512\n" \
  "ds_read_b64_tr_b16 v[12:13], v110 offset:8192\n" \
  "ds_read_b64_tr_b16 v[14:15], v112 offset:8704\n" \
  "ds_read_b64_tr_b16 v[24:25], v110 offset:8\n" \
  "ds_read_b64_tr_b16 v[26:27], v112 offset:520\n" \
  "ds_read_b64_tr_b16 v[28:29], v110 offset:8200\n" \
  "ds_read_b64_tr_b16 v[30:31], v112 offset:8712\n" \
  "ds_read_b64_tr_b16 v[40:41], v111 offset:0\n" \
  "ds_read_b64_tr_b16 v[42:43], v113 offset:512\n" \
  "ds_read_b64_tr_b16 v[44:45], v111 offset:8192\n" \
  "ds_read_b64_tr_b16 v[46:47], v113 offset:8704\n" \
  "ds_read_b64_tr_b16 v[56:57], v111 offset:8\n" \
  "ds_read_b64_tr_b16 v[58:59], v113 offset:520\n" \
  "ds_read_b64_tr_b16 v[60:61], v111 offset:8200\n" \
  "s_waitcnt lgkmcnt(8)\n" \
  "ds_read_b64_tr_b16 v[62:63], v113 offset:8712\n" \
  "nowo18_%=:\n" \
  "s_waitcnt vmcnt(32)\n" \
  "s_waitcnt lgkmcnt(0)\n" \
  "s_barrier\n" \
  "s_lshr_b32 s84, %[role], 5\n" \
  "s_and_b32 s84, s84, 1\n" \
  "s_cmp_eq_u32 s84, 1\n" \
  "s_cbranch_scc0 nowo19_%=\n" \
  "v_add_u32 v110, 147456, v252\n" \
  "v_add_u32 v111, 147456, v253\n" \
  "v_add_u32 v112, 147456, v254\n" \
  "v_add_u32 v113, 147456, v255\n" \
  "ds_read_b64_tr_b16 v[8:9], v110 offset:0\n" \
  "ds_read_b64_tr_b16 v[10:11], v112 offset:512\n" \
  "ds_read_b64_tr_b16 v[12:13], v110 offset:8192\n" \
  "ds_read_b64_tr_b16 v[14:15], v112 offset:8704\n" \
  "ds_read_b64_tr_b16 v[24:25], v110 offset:8\n" \
  "ds_read_b64_tr_b16 v[26:27], v112 offset:520\n" \
  "ds_read_b64_tr_b16 v[28:29], v110 offset:8200\n" \
  "ds_read_b64_tr_b16 v[30:31], v112 offset:8712\n" \
  "ds_read_b64_tr_b16 v[40:41], v111 offset:0\n" \
  "ds_read_b64_tr_b16 v[42:43], v113 offset:512\n" \
  "ds_read_b64_tr_b16 v[44:45], v111 offset:8192\n" \
  "ds_read_b64_tr_b16 v[46:47], v113 offset:8704\n" \
  "ds_read_b64_tr_b16 v[56:57], v111 offset:8\n" \
  "ds_read_b64_tr_b16 v[58:59], v113 offset:520\n" \
  "ds_read_b64_tr_b16 v[60:61], v111 offset:8200\n" \
  "s_waitcnt lgkmcnt(8)\n" \
  "ds_read_b64_tr_b16 v[62:63], v113 offset:8712\n" \
  "nowo19_%=:\n" \
  "s_waitcnt vmcnt(28)\n" \
  "s_waitcnt lgkmcnt(0)\n" \
  "s_barrier\n" \
  "s_waitcnt vmcnt(24)\n" \
  "s_waitcnt lgkmcnt(0)\n" \
  "s_barrier\n" \
  "s_waitcnt vmcnt(20)\n" \
  "s_waitcnt lgkmcnt(0)\n" \
  "s_barrier\n" \
  "s_waitcnt vmcnt(16)\n" \
  "s_waitcnt lgkmcnt(0)\n" \
  "s_barrier\n" \
  "s_waitcnt vmcnt(12)\n" \
  "s_waitcnt lgkmcnt(0)\n" \
  "s_barrier\n" \
  "s_waitcnt vmcnt(8)\n" \
  "s_waitcnt lgkmcnt(0)\n" \
  "s_barrier\n" \
  "s_waitcnt vmcnt(4)\n" \
  "s_waitcnt lgkmcnt(0)\n" \
  "s_barrier\n" \
  "s_waitcnt vmcnt(0)\n" \
  "s_waitcnt lgkmcnt(0)\n" \
  "s_barrier\n" \
  "s_mov_b32 s84, %[wv]\n" \
  "s_lshl_b32 s84, s84, 14\n" \
  "v_add_u32 v90, s84, v250\n" \
  "ds_read_b64_tr_b16 v[92:93], v90 offset:0\n" \
  "ds_read_b64_tr_b16 v[94:95], v90 offset:2080\n" \
  "ds_read_b64_tr_b16 v[96:97], v90 offset:8192\n" \
  "ds_read_b64_tr_b16 v[98:99], v90 offset:10272\n" \
  "ds_read_b64_tr_b16 v[100:101], v90 offset:32\n" \
  "ds_read_b64_tr_b16 v[102:103], v90 offset:2048\n" \
  "ds_read_b64_tr_b16 v[104:105], v90 offset:8224\n" \
  "ds_read_b64_tr_b16 v[106:107], v90 offset:10240\n" \
  "s_add_u32 s84, %[wv], 4\n" \
  "s_lshl_b32 s84, s84, 14\n" \
  "v_add_u32 v90, s84, v250\n" \
  "ds_read_b64_tr_b16 v[108:109], v90 offset:0\n" \
  "ds_read_b64_tr_b16 v[110:111], v90 offset:2080\n" \
  "ds_read_b64_tr_b16 v[112:113], v90 offset:8192\n" \
  "ds_read_b64_tr_b16 v[114:115], v90 offset:10272\n" \
  "ds_read_b64_tr_b16 v[116:117], v90 offset:32\n" \
  "ds_read_b64_tr_b16 v[118:119], v90 offset:2048\n" \
  "ds_read_b64_tr_b16 v[120:121], v90 offset:8224\n" \
  "s_waitcnt lgkmcnt(8)\n" \
  "ds_read_b64_tr_b16 v[122:123], v90 offset:10240\n" \
  "s_add_u32 s84, %[wv], 1\n" \
  "s_and_b32 s84, s84, 3\n" \
  "s_lshl_b32 s84, s84, 14\n" \
  "v_add_u32 v90, s84, v250\n" \
  "ds_read_b64_tr_b16 v[124:125], v90 offset:0\n" \
  "ds_read_b64_tr_b16 v[126:127], v90 offset:2080\n" \
  "ds_read_b64_tr_b16 v[128:129], v90 offset:8192\n" \
  "ds_read_b64_tr_b16 v[130:131], v90 offset:10272\n" \
  "ds_read_b64_tr_b16 v[132:133], v90 offset:32\n" \
  "ds_read_b64_tr_b16 v[134:135], v90 offset:2048\n" \
  "s_waitcnt lgkmcnt(8)\n" \
  "ds_read_b64_tr_b16 v[136:137], v90 offset:8224\n" \
  "ds_read_b64_tr_b16 v[138:139], v90 offset:10240\n" \
  "s_add_u32 s84, %[wv], 2\n" \
  "s_and_b32 s84, s84, 3\n" \
  "s_lshl_b32 s84, s84, 14\n" \
  "v_add_u32 v90, s84, v250\n" \
  "ds_read_b64_tr_b16 v[140:141], v90 offset:0\n" \
  "ds_read_b64_tr_b16 v[142:143], v90 offset:2080\n" \
  "ds_read_b64_tr_b16 v[144:145], v90 offset:8192\n" \
  "ds_read_b64_tr_b16 v[146:147], v90 offset:10272\n" \
  "ds_read_b64_tr_b16 v[148:149], v90 offset:32\n" \
  "s_waitcnt lgkmcnt(8)\n" \
  "ds_read_b64_tr_b16 v[150:151], v90 offset:2048\n" \
  "ds_read_b64_tr_b16 v[152:153], v90 offset:8224\n" \
  "ds_read_b64_tr_b16 v[154:155], v90 offset:10240\n" \
  "s_add_u32 s84, %[wv], 3\n" \
  "s_and_b32 s84, s84, 3\n" \
  "s_lshl_b32 s84, s84, 14\n" \
  "v_add_u32 v90, s84, v250\n" \
  "ds_read_b64_tr_b16 v[156:157], v90 offset:0\n" \
  "ds_read_b64_tr_b16 v[158:159], v90 offset:2080\n" \
  "ds_read_b64_tr_b16 v[160:161], v90 offset:8192\n" \
  "ds_read_b64_tr_b16 v[162:163], v90 offset:10272\n" \
  "s_waitcnt lgkmcnt(8)\n" \
  "ds_read_b64_tr_b16 v[164:165], v90 offset:32\n" \
  "ds_read_b64_tr_b16 v[166:167], v90 offset:2048\n" \
  "ds_read_b64_tr_b16 v[168:169], v90 offset:8224\n" \
  "ds_read_b64_tr_b16 v[170:171], v90 offset:10240\n" \
  "s_add_u32 s84, %[wv], 1\n" \
  "s_and_b32 s84, s84, 3\n" \
  "s_add_u32 s84, s84, 4\n" \
  "s_lshl_b32 s84, s84, 14\n" \
  "v_add_u32 v90, s84, v250\n" \
  "ds_read_b64_tr_b16 v[172:173], v90 offset:0\n" \
  "ds_read_b64_tr_b16 v[174:175], v90 offset:2080\n" \
  "ds_read_b64_tr_b16 v[176:177], v90 offset:8192\n" \
  "s_waitcnt lgkmcnt(8)\n" \
  "ds_read_b64_tr_b16 v[178:179], v90 offset:10272\n" \
  "ds_read_b64_tr_b16 v[180:181], v90 offset:32\n" \
  "ds_read_b64_tr_b16 v[182:183], v90 offset:2048\n" \
  "ds_read_b64_tr_b16 v[184:185], v90 offset:8224\n" \
  "ds_read_b64_tr_b16 v[186:187], v90 offset:10240\n" \
  "s_add_u32 s84, %[wv], 2\n" \
  "s_and_b32 s84, s84, 3\n" \
  "s_add_u32 s84, s84, 4\n" \
  "s_lshl_b32 s84, s84, 14\n" \
  "v_add_u32 v90, s84, v250\n" \
  "ds_read_b64_tr_b16 v[188:189], v90 offset:0\n" \
  "ds_read_b64_tr_b16 v[190:191], v90 offset:2080\n" \
  "s_waitcnt lgkmcnt(8)\n" \
  "ds_read_b64_tr_b16 v[192:193], v90 offset:8192\n" \
  "ds_read_b64_tr_b16 v[194:195], v90 offset:10272\n" \
  "ds_read_b64_tr_b16 v[196:197], v90 offset:32\n" \
  "ds_read_b64_tr_b16 v[198:199], v90 offset:2048\n" \
  "ds_read_b64_tr_b16 v[200:201], v90 offset:8224\n" \
  "ds_read_b64_tr_b16 v[202:203], v90 offset:10240\n" \
  "s_add_u32 s84, %[wv], 3\n" \
  "s_and_b32 s84, s84, 3\n" \
  "s_add_u32 s84, s84, 4\n" \
  "s_lshl_b32 s84, s84, 14\n" \
  "v_add_u32 v90, s84, v250\n" \
  "ds_read_b64_tr_b16 v[204:205], v90 offset:0\n" \
  "s_waitcnt lgkmcnt(8)\n" \
  "ds_read_b64_tr_b16 v[206:207], v90 offset:2080\n" \
  "ds_read_b64_tr_b16 v[208:209], v90 offset:8192\n" \
  "ds_read_b64_tr_b16 v[210:211], v90 offset:10272\n" \
  "ds_read_b64_tr_b16 v[212:213], v90 offset:32\n" \
  "ds_read_b64_tr_b16 v[214:215], v90 offset:2048\n" \
  "ds_read_b64_tr_b16 v[216:217], v90 offset:8224\n" \
  "ds_read_b64_tr_b16 v[218:219], v90 offset:10240\n" \
  "s_waitcnt lgkmcnt(0)\n" \
  "s_barrier\n" \
  "ds_write_b128 %[lv0], v[92:95] offset:0\n" \
  "ds_write_b128 %[lv0], v[96:99] offset:1024\n" \
  "ds_write_b128 %[lv0], v[100:103] offset:2048\n" \
  "ds_write_b128 %[lv0], v[104:107] offset:3072\n" \
  "ds_write_b128 %[lv0], v[108:111] offset:16384\n" \
  "ds_write_b128 %[lv0], v[112:115] offset:17408\n" \
  "ds_write_b128 %[lv0], v[116:119] offset:18432\n" \
  "ds_write_b128 %[lv0], v[120:123] offset:19456\n" \
  "ds_write_b128 %[lv0], v[124:127] offset:32768\n" \
  "ds_write_b128 %[lv0], v[128:131] offset:33792\n" \
  "ds_write_b128 %[lv0], v[132:135] offset:34816\n" \
  "ds_write_b128 %[lv0], v[136:139] offset:35840\n" \
  "ds_write_b128 %[lv0], v[140:143] offset:49152\n" \
  "ds_write_b128 %[lv0], v[144:147] offset:50176\n" \
  "ds_write_b128 %[lv0], v[148:151] offset:51200\n" \
  "s_waitcnt lgkmcnt(8)\n" \
  "ds_write_b128 %[lv0], v[152:155] offset:52224\n" \
  "ds_write_b128 %[lv1], v[156:159] offset:0\n" \
  "ds_write_b128 %[lv1], v[160:163] offset:1024\n" \
  "ds_write_b128 %[lv1], v[164:167] offset:2048\n" \
  "ds_write_b128 %[lv1], v[168:171] offset:3072\n" \
  "ds_write_b128 %[lv1], v[172:175] offset:16384\n" \
  "ds_write_b128 %[lv1], v[176:179] offset:17408\n" \
  "s_waitcnt lgkmcnt(8)\n" \
  "ds_write_b128 %[lv1], v[180:183] offset:18432\n" \
  "ds_write_b128 %[lv1], v[184:187] offset:19456\n" \
  "ds_write_b128 %[lv1], v[188:191] offset:32768\n" \
  "ds_write_b128 %[lv1], v[192:195] offset:33792\n" \
  "ds_write_b128 %[lv1], v[196:199] offset:34816\n" \
  "ds_write_b128 %[lv1], v[200:203] offset:35840\n" \
  "ds_write_b128 %[lv1], v[204:207] offset:49152\n" \
  "s_waitcnt lgkmcnt(8)\n" \
  "ds_write_b128 %[lv1], v[208:211] offset:50176\n" \
  "ds_write_b128 %[lv1], v[212:215] offset:51200\n" \
  "ds_write_b128 %[lv1], v[216:219] offset:52224\n" \
  "s_waitcnt lgkmcnt(0)\n" \
  "s_barrier\n" \
  "s_mov_b32 s64, 0\n" \
  "s_mov_b32 s71, 0\n" \
  "s_mov_b32 s58, 0\n" \
  "s_mov_b32 s73, 0\n" \
  "s_mov_b32 s77, %[abo]\n" \
  "s_mov_b32 s88, s60\n" \
  "s_mov_b32 s91, s61\n" \
  "s_lshl_b32 s88, s88, 13\n" \
  "s_lshl_b32 s91, s91, 13\n" \
  "s_add_u32 s74, %[gb], s88\n" \
  "s_add_u32 s75, s74, 4096\n" \
  "s_add_u32 s62, %[rsb], s91\n" \
  "s_add_u32 s63, %[rrb], s91\n" \
  "s_add_u32 s59, %[flkm], s88\n" \
  "s_mov_b32 s80, %[kb]\n" \
  "s_add_u32 s81, %[kb], 4096\n" \
  "s_mov_b32 s82, %[vb]\n" \
  "s_mov_b32 s83, %[kmb]\n" \
  "s_add_u32 s86, %[gs0], 16384\n" \
  "s_add_u32 s86, s86, s88\n" \
  "s_add_u32 s87, %[stb], s88\n" \
  "v_mov_b32 v233, 0\n" \
  "v_mov_b32 v234, 0\n" \
  "v_mov_b32 v184, 0\n" \
  "v_mov_b32 v185, 0\n" \
  "v_mov_b32 v228, 0x3f803f80\n" \
  "v_mov_b32 v229, 0x3f803f80\n" \
  "v_mov_b32 v230, 0x3f803f80\n" \
  "v_mov_b32 v231, 0x3f803f80\n" \
  "v_add_u32 v255, %[oab], %[l16]\n" \
  "s_lshl_b32 s68, %[wv], 9\n" \
  "v_add_u32 v254, s68, %[smr]\n" \
  "s_memrealtime s[66:67]\n" \
  "s_waitcnt lgkmcnt(0)\n" \
  "s_mov_b32 s65, s66\n" \
  "step_%=:\n" \
  "wg_%=:\n" \
  "s_memrealtime s[66:67]\n" \
  "s_waitcnt lgkmcnt(0)\n" \
  "s_sub_u32 s68, s66, s65\n" \
  "s_cmp_lt_i32 s68, 0\n" \
  "s_cbranch_scc1 wg_%=\n" \
  "buffer_load_dwordx4 v[92:95], %[vg], %[rs], s74 offen sc1\n" \
  "s_mov_b32 s57, 0\n" \
  "v_mov_b64 v[188:189], 0\n" \
  "v_mov_b64 v[190:191], 0\n" \
  "v_mov_b64 v[192:193], 0\n" \
  "v_mov_b64 v[194:195], 0\n" \
  "v_mov_b64 v[196:197], 0\n" \
  "v_mov_b64 v[198:199], 0\n" \
  "v_mov_b64 v[200:201], 0\n" \
  "v_mov_b64 v[202:203], 0\n" \
  "v_mov_b64 v[204:205], 0\n" \
  "v_mov_b64 v[206:207], 0\n" \
  "v_mov_b64 v[208:209], 0\n" \
  "v_mov_b64 v[210:211], 0\n" \
  "s_and_b32 s68, s64, 1\n" \
  "s_lshl_b32 s68, s68, 19\n" \
  "s_add_u32 s84, s62, s68\n" \
  "s_add_u32 s85, s63, s68\n" \
  "s_nop 7\n" \
  "buffer_load_dwordx4 v[96:99], %[vg], %[rs], s75 offen sc1\n" \
  "s_waitcnt vmcnt(2)\n" \
  "v_readfirstlane_b32 s68, v184\n" \
  "v_readfirstlane_b32 s69, v185\n" \
  "s_nop 1\n" \
  "s_max_u32 s71, s71, s68\n" \
  "s_max_u32 s58, s58, s69\n" \
  "s_lshr_b32 s69, s64, 1\n" \
  "s_cmp_le_u32 s71, s69\n" \
  "s_cbranch_scc1 kv_%=\n" \
  "kvok_%=:\n" \
  "buffer_load_dwordx4 v[212:215], %[l16], %[rs], s80 offen sc1\n" \
  "buffer_load_dwordx4 v[216:219], %[l16], %[rs], s81 offen sc1\n" \
  "s_add_u32 s80, s80, 8192\n" \
  "s_add_u32 s81, s81, 8192\n" \
  "s_waitcnt vmcnt(3)\n" \
  "v_max3_u32 v90, v92, v93, v94\n" \
  "v_max_u32 v90, v90, v95\n" \
  "v_cmp_eq_u32 vcc, -1, v90\n" \
  "s_cbranch_vccnz sg0_%=\n" \
  "g0ok_%=:\n" \
  "v_smfmac_f32_16x16x64_bf16 v[188:191], v[92:95], a[0:7], %[idx]\n" \
  "ds_write_b128 %[lw], v[92:95] offset:0\n" \
  "v_smfmac_f32_16x16x64_bf16 v[196:199], v[92:95], a[128:135], %[idx]\n" \
  "ds_read_b128 v[124:127], %[lv0] offset:0\n" \
  "v_smfmac_f32_16x16x64_bf16 v[192:195], v[92:95], a[64:71], %[idx]\n" \
  "ds_read_b128 v[128:131], %[lv0] offset:1024\n" \
  "v_smfmac_f32_16x16x64_bf16 v[200:203], v[92:95], a[192:199], %[idx]\n" \
  "ds_read_b128 v[132:135], %[lv0] offset:2048\n" \
  "ds_read_b128 v[136:139], %[lv0] offset:3072\n" \
  "s_waitcnt vmcnt(2)\n" \
  "v_max3_u32 v90, v96, v97, v98\n" \
  "v_max_u32 v90, v90, v99\n" \
  "v_cmp_eq_u32 vcc, -1, v90\n" \
  "s_cbranch_vccnz sg1_%=\n" \
  "g1ok_%=:\n" \
  "v_smfmac_f32_16x16x64_bf16 v[188:191], v[96:99], a[8:15], %[idx]\n" \
  "ds_write_b128 %[lw], v[96:99] offset:4096\n" \
  "v_smfmac_f32_16x16x64_bf16 v[196:199], v[96:99], a[136:143], %[idx]\n" \
  "ds_read_b128 v[140:143], %[lv0] offset:16384\n" \
  "v_smfmac_f32_16x16x64_bf16 v[192:195], v[96:99], a[72:79], %[idx]\n" \
  "ds_read_b128 v[144:147], %[lv0] offset:17408\n" \
  "v_smfmac_f32_16x16x64_bf16 v[200:203], v[96:99], a[200:207], %[idx]\n" \
  "ds_read_b128 v[148:151], %[lv0] offset:18432\n" \
  "ds_read_b128 v[152:155], %[lv0] offset:19456\n" \
  "s_waitcnt lgkmcnt(4)\n" \
  "s_barrier\n" \
  "ds_read_b128 v[100:103], %[la1] offset:0\n" \
  "ds_read_b128 v[104:107], %[la2] offset:0\n" \
  "ds_read_b128 v[108:111], %[la3] offset:0\n" \
  "v_smfmac_f32_16x16x64_bf16 v[204:207], v[92:95], v[124:131], %[idx]\n" \
  "ds_read_b128 v[112:115], %[la1] offset:4096\n" \
  "v_smfmac_f32_16x16x64_bf16 v[208:211], v[92:95], v[132:139], %[idx]\n" \
  "ds_read_b128 v[116:119], %[la2] offset:4096\n" \
  "s_waitcnt lgkmcnt(7)\n" \
  "v_smfmac_f32_16x16x64_bf16 v[204:207], v[96:99], v[140:147], %[idx]\n" \
  "ds_read_b128 v[120:123], %[la3] offset:4096\n" \
  "s_waitcnt lgkmcnt(6)\n" \
  "v_smfmac_f32_16x16x64_bf16 v[208:211], v[96:99], v[148:155], %[idx]\n" \
  "ds_read_b128 v[156:159], %[lv0] offset:32768\n" \
  "s_waitcnt lgkmcnt(6)\n" \
  "v_smfmac_f32_16x16x64_bf16 v[188:191], v[100:103], a[16:23], %[idx]\n" \
  "ds_read_b128 v[160:163], %[lv0] offset:33792\n" \
  "v_smfmac_f32_16x16x64_bf16 v[196:199], v[100:103], a[144:151], %[idx]\n" \
  "ds_read_b128 v[164:167], %[lv0] offset:34816\n" \
  "v_smfmac_f32_16x16x64_bf16 v[192:195], v[100:103], a[80:87], %[idx]\n" \
  "ds_read_b128 v[168:171], %[lv0] offset:35840\n" \
  "v_smfmac_f32_16x16x64_bf16 v[200:203], v[100:103], a[208:215], %[idx]\n" \
  "s_waitcnt lgkmcnt(8)\n" \
  "v_smfmac_f32_16x16x64_bf16 v[188:191], v[104:107], a[24:31], %[idx]\n" \
  "ds_read_b128 v[172:175], %[lv0] offset:49152\n" \
  "v_smfmac_f32_16x16x64_bf16 v[196:199], v[104:107], a[152:159], %[idx]\n" \
  "ds_read_b128 v[176:179], %[lv0] offset:50176\n" \
  "v_smfmac_f32_16x16x64_bf16 v[192:195], v[104:107], a[88:95], %[idx]\n" \
  "ds_read_b128 v[180:183], %[lv0] offset:51200\n" \
  "v_smfmac_f32_16x16x64_bf16 v[200:203], v[104:107], a[216:223], %[idx]\n" \
  "s_waitcnt lgkmcnt(10)\n" \
  "v_smfmac_f32_16x16x64_bf16 v[188:191], v[108:111], a[32:39], %[idx]\n" \
  "ds_read_b128 v[184:187], %[lv0] offset:52224\n" \
  "v_smfmac_f32_16x16x64_bf16 v[196:199], v[108:111], a[160:167], %[idx]\n" \
  "ds_read_b128 v[124:127], %[lv1] offset:0\n" \
  "v_smfmac_f32_16x16x64_bf16 v[192:195], v[108:111], a[96:103], %[idx]\n" \
  "ds_read_b128 v[128:131], %[lv1] offset:1024\n" \
  "v_smfmac_f32_16x16x64_bf16 v[200:203], v[108:111], a[224:231], %[idx]\n" \
  "s_waitcnt lgkmcnt(12)\n" \
  "v_smfmac_f32_16x16x64_bf16 v[188:191], v[112:115], a[40:47], %[idx]\n" \
  "ds_read_b128 v[132:135], %[lv1] offset:2048\n" \
  "v_smfmac_f32_16x16x64_bf16 v[196:199], v[112:115], a[168:175], %[idx]\n" \
  "ds_read_b128 v[136:139], %[lv1] offset:3072\n" \
  "v_smfmac_f32_16x16x64_bf16 v[192:195], v[112:115], a[104:111], %[idx]\n" \
  "ds_read_b128 v[140:143], %[lv1] offset:16384\n" \
  "v_smfmac_f32_16x16x64_bf16 v[200:203], v[112:115], a[232:239], %[idx]\n" \
  "s_waitcnt lgkmcnt(14)\n" \
  "v_smfmac_f32_16x16x64_bf16 v[188:191], v[116:119], a[48:55], %[idx]\n" \
  "ds_read_b128 v[144:147], %[lv1] offset:17408\n" \
  "v_smfmac_f32_16x16x64_bf16 v[196:199], v[116:119], a[176:183], %[idx]\n" \
  "s_waitcnt lgkmcnt(8)\n" \
  "ds_read_b128 v[148:151], %[lv1] offset:18432\n" \
  "v_smfmac_f32_16x16x64_bf16 v[192:195], v[116:119], a[112:119], %[idx]\n" \
  "ds_read_b128 v[152:155], %[lv1] offset:19456\n" \
  "v_smfmac_f32_16x16x64_bf16 v[200:203], v[116:119], a[240:247], %[idx]\n" \
  "s_lshr_b32 s69, s64, 1\n" \
  "s_cmp_le_u32 s58, s69\n" \
  "s_cbranch_scc1 vv_%=\n" \
  "vvok_%=:\n" \
  "v_smfmac_f32_16x16x64_bf16 v[188:191], v[120:123], a[56:63], %[idx]\n" \
  "buffer_load_dwordx4 v[220:223], %[l16], %[rs], s82 offen sc1\n" \
  "v_smfmac_f32_16x16x64_bf16 v[192:195], v[120:123], a[120:127], %[idx]\n" \
  "buffer_load_dwordx4 v[224:227], %[l16], %[rs], s82 offen offset:1024 sc1\n" \
  "v_smfmac_f32_16x16x64_bf16 v[196:199], v[120:123], a[184:191], %[idx]\n" \
  "s_add_u32 s82, s82, 8192\n" \
  "v_smfmac_f32_16x16x64_bf16 v[200:203], v[120:123], a[248:255], %[idx]\n" \
  "v_smfmac_f32_16x16x64_bf16 v[204:207], v[100:103], v[156:163], %[idx]\n" \
  "v_smfmac_f32_16x16x64_bf16 v[208:211], v[100:103], v[164:171], %[idx]\n" \
  "ds_read_b128 v[156:159], %[lv1] offset:32768\n" \
  "v_pk_add_f32 v[188:189], v[188:189], v[190:191]\n" \
  "v_pk_add_f32 v[192:193], v[192:193], v[194:195]\n" \
  "v_cvt_pk_bf16_f32 v242, v188, v189\n" \
  "v_cvt_pk_bf16_f32 v243, v192, v193\n" \
  "v_pk_add_f32 v[196:197], v[196:197], v[198:199]\n" \
  "v_pk_add_f32 v[200:201], v[200:201], v[202:203]\n" \
  "v_cvt_pk_bf16_f32 v244, v196, v197\n" \
  "v_cvt_pk_bf16_f32 v245, v200, v201\n" \
  "v_mfma_f32_16x16x16_bf16 v[188:191], v[242:243], %[sq0], 0\n" \
  "ds_read_b128 v[160:163], %[lv1] offset:33792\n" \
  "v_mfma_f32_16x16x16_bf16 v[192:195], v[242:243], %[sq1], 0\n" \
  "ds_read_b128 v[164:167], %[lv1] offset:34816\n" \
  "v_mfma_f32_16x16x16_bf16 v[196:199], v[244:245], %[sk0], 0\n" \
  "ds_read_b128 v[168:171], %[lv1] offset:35840\n" \
  "v_mfma_f32_16x16x16_bf16 v[200:203], v[244:245], %[sk1], 0\n" \
  "v_smfmac_f32_16x16x64_bf16 v[204:207], v[104:107], v[172:179], %[idx]\n" \
  "s_waitcnt lgkmcnt(12)\n" \
  "v_smfmac_f32_16x16x64_bf16 v[208:211], v[104:107], v[180:187], %[idx]\n" \
  "ds_read_b128 v[172:175], %[lv1] offset:49152\n" \
  "v_cvt_pk_bf16_f32 v246, v188, v189\n" \
  "v_cvt_pk_bf16_f32 v247, v190, v191\n" \
  "v_cvt_pk_bf16_f32 v248, v192, v193\n" \
  "v_cvt_pk_bf16_f32 v249, v194, v195\n" \
  "v_cvt_pk_bf16_f32 v236, v196, v197\n" \
  "v_cvt_pk_bf16_f32 v237, v198, v199\n" \
  "v_cvt_pk_bf16_f32 v238, v200, v201\n" \
  "v_cvt_pk_bf16_f32 v239, v202, v203\n" \
  "s_waitcnt vmcnt(3)\n" \
  "v_mfma_f32_16x16x32_bf16 v[188:191], v[212:215], v[246:249], 0\n" \
  "ds_read_b128 v[176:179], %[lv1] offset:50176\n" \
  "s_waitcnt vmcnt(2)\n" \
  "v_mfma_f32_16x16x32_bf16 v[192:195], v[216:219], v[246:249], 0\n" \
  "ds_read_b128 v[180:183], %[lv1] offset:51200\n" \
  "v_mfma_f32_16x16x32_bf16 v[196:199], v[236:239], v[246:249], 0\n" \
  "s_waitcnt lgkmcnt(8)\n" \
  "ds_read_b128 v[184:187], %[lv1] offset:52224\n" \
  "v_smfmac_f32_16x16x64_bf16 v[204:207], v[108:111], v[124:131], %[idx]\n" \
  "v_smfmac_f32_16x16x64_bf16 v[208:211], v[108:111], v[132:139], %[idx]\n" \
  "ds_write_b128 %[scw], v[188:191] offset:0\n" \
  "ds_write_b128 %[scw], v[192:195] offset:1024\n" \
  "ds_write_b64 v254, v[196:197] offset:0\n" \
  "s_memrealtime s[66:67]\n" \
  "s_cmp_eq_u32 s64, 63\n" \
  "s_waitcnt lgkmcnt(0)\n" \
  "s_barrier\n" \
  "s_cbranch_scc1 last_%=\n" \
  "ds_read_b128 v[92:95], %[scx] offset:0\n" \
  "ds_read_b128 v[96:99], %[scx] offset:2048\n" \
  "ds_read_b128 v[100:103], %[scx] offset:4096\n" \
  "ds_read_b128 v[104:107], %[scx] offset:6144\n" \
  "ds_read_b64 v[188:189], %[smr] offset:0\n" \
  "ds_read_b64 v[190:191], %[smr] offset:512\n" \
  "ds_read_b64 v[192:193], %[smr] offset:1024\n" \
  "ds_read_b64 v[194:195], %[smr] offset:1536\n" \
  "v_smfmac_f32_16x16x64_bf16 v[204:207], v[112:115], v[140:147], %[idx]\n" \
  "v_mov_b64 v[212:213], 0\n" \
  "v_smfmac_f32_16x16x64_bf16 v[208:211], v[112:115], v[148:155], %[idx]\n" \
  "v_mov_b64 v[214:215], 0\n" \
  "v_smfmac_f32_16x16x64_bf16 v[204:207], v[116:119], v[156:163], %[idx]\n" \
  "v_mov_b64 v[216:217], 0\n" \
  "v_smfmac_f32_16x16x64_bf16 v[208:211], v[116:119], v[164:171], %[idx]\n" \
  "v_mov_b64 v[218:219], 0\n" \
  "v_smfmac_f32_16x16x64_bf16 v[204:207], v[120:123], v[172:179], %[idx]\n" \
  "v_smfmac_f32_16x16x64_bf16 v[208:211], v[120:123], v[180:187], %[idx]\n" \
  "s_bitcmp1_b32 %[role], 1\n" \
  "s_cbranch_scc0 nost_%=\n" \
  "s_add_u32 s68, s66, s57\n" \
  "s_min_u32 s68, s68, -2\n" \
  "v_mov_b32 v90, s68\n" \
  "s_mov_b64 exec, 1\n" \
  "buffer_store_dword v90, off, %[rs], s87 sc1\n" \
  "s_mov_b64 exec, -1\n" \
  "nost_%=:\n" \
  "s_waitcnt lgkmcnt(6)\n" \
  "v_pk_add_f32 v[92:93], v[92:93], v[96:97]\n" \
  "v_pk_add_f32 v[94:95], v[94:95], v[98:99]\n" \
  "s_waitcnt lgkmcnt(4)\n" \
  "v_pk_add_f32 v[100:101], v[100:101], v[104:105]\n" \
  "v_pk_add_f32 v[102:103], v[102:103], v[106:107]\n" \
  "s_waitcnt lgkmcnt(2)\n" \
  "v_pk_add_f32 v[188:189], v[188:189], v[190:191]\n" \
  "s_waitcnt lgkmcnt(0)\n" \
  "v_pk_add_f32 v[192:193], v[192:193], v[194:195]\n" \
  "v_pk_add_f32 v[92:93], v[92:93], v[100:101]\n" \
  "v_pk_add_f32 v[94:95], v[94:95], v[102:103]\n" \
  "v_pk_add_f32 v[188:189], v[188:189], v[192:193]\n" \
  "v_max3_f32 v251, v92, v93, v94\n" \
  "v_max3_f32 v252, v95, v188, v189\n" \
  "v_max_f32 v251, v251, v252\n" \
  "s_nop 1\n" \
  "v_max_f32_dpp v251, v251, v251 row_ror:8 row_mask:0xf bank_mask:0xf\n" \
  "v_mov_b32 v252, v251\n" \
  "s_nop 1\n" \
  "v_permlane16_swap_b32 v252, v251\n" \
  "v_max_f32 v251, v251, v252\n" \
  "v_mov_b32 v252, v251\n" \
  "s_nop 1\n" \
  "v_permlane32_swap_b32 v252, v251\n" \
  "v_max_f32 v251, v251, v252\n" \
  "v_mul_f32 v253, %[cs], v251\n" \
  "v_fma_f32 v92, v92, %[cs], -v253\n" \
  "v_fma_f32 v93, v93, %[cs], -v253\n" \
  "v_fma_f32 v94, v94, %[cs], -v253\n" \
  "v_fma_f32 v95, v95, %[cs], -v253\n" \
  "v_fma_f32 v188, v188, %[cs], -v253\n" \
  "v_fma_f32 v189, v189, %[cs], -v253\n" \
  "v_exp_f32 v92, v92\n" \
  "v_exp_f32 v93, v93\n" \
  "v_exp_f32 v94, v94\n" \
  "v_exp_f32 v95, v95\n" \
  "v_exp_f32 v188, v188\n" \
  "v_exp_f32 v189, v189\n" \
  "v_pk_add_f32 v[204:205], v[204:205], v[206:207]\n" \
  "v_pk_add_f32 v[208:209], v[208:209], v[210:211]\n" \
  "v_cvt_pk_bf16_f32 v251, v92, v93\n" \
  "v_cvt_pk_bf16_f32 v252, v94, v95\n" \
  "v_cvt_pk_bf16_f32 v253, v188, v189\n" \
  "v_cvt_pk_bf16_f32 v240, v204, v205\n" \
  "v_cvt_pk_bf16_f32 v241, v208, v209\n" \
  "v_cndmask_b32_e64 v246, 0, v251, %[mlo]\n" \
  "v_cndmask_b32_e64 v247, 0, v252, %[mlo]\n" \
  "v_cndmask_b32_e64 v248, v251, 0, %[mlo]\n" \
  "v_cndmask_b32_e64 v249, v252, 0, %[mlo]\n" \
  "v_cndmask_b32_e64 v232, 0, v253, %[mlo]\n" \
  "v_cndmask_b32_e64 v235, 0, v253, %[mlo]\n" \
  "v_mfma_f32_16x16x32_bf16 v[188:191], v[228:231], v[246:249], 0\n" \
  "buffer_load_dword v250, off, %[rs], s87  sc1\n" \
  "s_waitcnt vmcnt(2)\n" \
  "v_mfma_f32_16x16x32_bf16 v[196:199], v[220:223], v[246:249], 0\n" \
  "s_waitcnt vmcnt(1)\n" \
  "v_mfma_f32_16x16x32_bf16 v[200:203], v[224:227], v[246:249], 0\n" \
  "v_mfma_f32_16x16x16_bf16 v[188:191], v[228:229], v[232:233], v[188:191]\n" \
  "v_mov_b64 v[220:221], 0\n" \
  "v_mfma_f32_16x16x16_bf16 v[196:199], v[240:241], v[232:233], v[196:199]\n" \
  "v_mov_b64 v[222:223], 0\n" \
  "v_mfma_f32_16x16x16_bf16 v[200:203], v[240:241], v[234:235], v[200:203]\n" \
  "v_mov_b64 v[224:225], 0\n" \
  "v_mov_b64 v[226:227], 0\n" \
  "v_add_f32_dpp v188, v188, v188 row_ror:8 row_mask:0xf bank_mask:0xf\n" \
  "v_rcp_f32 v188, v188\n" \
  "s_nop 0\n" \
  "v_add_f32_dpp v196, v196, v196 row_ror:8 row_mask:0xf bank_mask:0xf\n" \
  "v_add_f32_dpp v197, v197, v197 row_ror:8 row_mask:0xf bank_mask:0xf\n" \
  "v_add_f32_dpp v198, v198, v198 row_ror:8 row_mask:0xf bank_mask:0xf\n" \
  "v_add_f32_dpp v199, v199, v199 row_ror:8 row_mask:0xf bank_mask:0xf\n" \
  "v_add_f32_dpp v200, v200, v200 row_ror:8 row_mask:0xf bank_mask:0xf\n" \
  "v_add_f32_dpp v201, v201, v201 row_ror:8 row_mask:0xf bank_mask:0xf\n" \
  "v_add_f32_dpp v202, v202, v202 row_ror:8 row_mask:0xf bank_mask:0xf\n" \
  "v_add_f32_dpp v203, v203, v203 row_ror:8 row_mask:0xf bank_mask:0xf\n" \
  "v_pk_mul_f32 v[196:197], v[196:197], v[188:189] op_sel_hi:[1,0]\n" \
  "v_pk_mul_f32 v[198:199], v[198:199], v[188:189] op_sel_hi:[1,0]\n" \
  "v_pk_mul_f32 v[200:201], v[200:201], v[188:189] op_sel_hi:[1,0]\n" \
  "v_pk_mul_f32 v[202:203], v[202:203], v[188:189] op_sel_hi:[1,0]\n" \
  "v_cvt_pk_bf16_f32 v242, v196, v197\n" \
  "v_cvt_pk_bf16_f32 v243, v198, v199\n" \
  "v_cvt_pk_bf16_f32 v244, v200, v201\n" \
  "v_cvt_pk_bf16_f32 v245, v202, v203\n" \
  "ds_write_b64 %[ow], v[242:243] offset:0\n" \
  "ds_write_b64 %[ow], v[244:245] offset:256\n" \
  "s_waitcnt lgkmcnt(0)\n" \
  "s_barrier\n" \
  "ds_read_b128 v[92:95], v255 offset:0\n" \
  "ds_read_b128 v[96:99], v255 offset:1024\n" \
  "s_waitcnt lgkmcnt(1)\n" \
  "v_smfmac_f32_16x16x64_bf16 v[212:215], v[92:95], v[0:7], %[idx]\n" \
  "v_smfmac_f32_16x16x64_bf16 v[216:219], v[92:95], v[16:23], %[idx]\n" \
  "v_smfmac_f32_16x16x64_bf16 v[220:223], v[92:95], v[32:39], %[idx]\n" \
  "v_smfmac_f32_16x16x64_bf16 v[224:227], v[92:95], v[48:55], %[idx]\n" \
  "s_waitcnt lgkmcnt(0)\n" \
  "v_smfmac_f32_16x16x64_bf16 v[212:215], v[96:99], v[8:15], %[idx]\n" \
  "v_smfmac_f32_16x16x64_bf16 v[216:219], v[96:99], v[24:31], %[idx]\n" \
  "v_smfmac_f32_16x16x64_bf16 v[220:223], v[96:99], v[40:47], %[idx]\n" \
  "v_smfmac_f32_16x16x64_bf16 v[224:227], v[96:99], v[56:63], %[idx]\n" \
  "s_nop 1\n" \
  "v_pk_add_f32 v[212:213], v[212:213], v[214:215]\n" \
  "s_nop 0\n" \
  "v_pk_add_f32 v[216:217], v[216:217], v[218:219]\n" \
  "v_cvt_pk_bf16_f32 v246, v212, v216\n" \
  "v_cvt_pk_bf16_f32 v248, v213, v217\n" \
  "v_pk_add_f32 v[220:221], v[220:221], v[222:223]\n" \
  "v_pk_add_f32 v[224:225], v[224:225], v[226:227]\n" \
  "v_cvt_pk_bf16_f32 v247, v220, v224\n" \
  "v_cvt_pk_bf16_f32 v249, v221, v225\n" \
  "buffer_store_dwordx4 v[246:249], %[rst], %[rs], s84 offen sc1\n" \
  "s_add_u32 s69, %[flq], 4096\n" \
  "buffer_load_dword v184, off, %[rs], %[flq]  sc1\n" \
  "buffer_load_dword v185, off, %[rs], s69  sc1\n" \
  "s_waitcnt vmcnt(3)\n" \
  "v_readfirstlane_b32 s76, v250\n" \
  "s_nop 0\n" \
  "s_cmp_eq_u32 s76, -1\n" \
  "s_cbranch_scc1 sst_%=\n" \
  "timed_%=:\n" \
  "s_add_u32 s65, s76, %[d2]\n" \
  "s_bitcmp1_b32 %[role], 2\n" \
  "s_cbranch_scc1 red_%=\n" \
  "s_bitcmp1_b32 %[role], 0\n" \
  "s_cbranch_scc1 pv0_%=\n" \
  "buffer_store_dwordx4 v[236:239], %[l16], %[rs], s83 offen sc1\n" \
  "s_branch pd0_%=\n" \
  "pv0_%=:\n" \
  "v_lshrrev_b32 v90, 1, %[l16]\n" \
  "buffer_store_dwordx2 v[240:241], v90, %[rs], s83 offen sc1\n" \
  "pd0_%=:\n" \
  "s_waitcnt vmcnt(0)\n" \
  "s_branch endb_%=\n" \
  "red_%=:\n" \
  "s_add_u32 s69, s76, %[d1]\n" \
  "v_and_b32 v152, 96, %[l16]\n" \
  "v_bfe_u32 v153, %[l16], 8, 1\n" \
  "v_lshlrev_b32 v153, 4, v153\n" \
  "v_add_u32 v152, v152, v153\n" \
  "wr_%=:\n" \
  "s_memrealtime s[66:67]\n" \
  "s_waitcnt lgkmcnt(0)\n" \
  "s_sub_u32 s68, s66, s69\n" \
  "s_cmp_lt_i32 s68, 0\n" \
  "s_cbranch_scc1 wr_%=\n" \
  "buffer_load_dwordx4 v[124:127], %[l16], %[rs], s85 offen sc1\n" \
  "buffer_load_dwordx4 v[128:131], %[l16], %[rs], s85 offen offset:1024 sc1\n" \
  "buffer_load_dwordx4 v[132:135], %[l16], %[rs], s85 offen offset:2048 sc1\n" \
  "buffer_load_dwordx4 v[136:139], %[l16], %[rs], s85 offen offset:3072 sc1\n" \
  "s_bitcmp1_b32 %[role], 0\n" \
  "s_cbranch_scc1 pv2_%=\n" \
  "buffer_store_dwordx4 v[236:239], %[l16], %[rs], s83 offen sc1\n" \
  "s_branch pd2_%=\n" \
  "pv2_%=:\n" \
  "v_lshrrev_b32 v90, 1, %[l16]\n" \
  "buffer_store_dwordx2 v[240:241], v90, %[rs], s83 offen sc1\n" \
  "pd2_%=:\n" \
  "s_waitcnt vmcnt(4)\n" \
  "v_mfma_f32_16x16x32_bf16 v[140:143], %[selr], v[124:127], 0\n" \
  "s_waitcnt vmcnt(3)\n" \
  "v_mfma_f32_16x16x32_bf16 v[140:143], %[selr], v[128:131], v[140:143]\n" \
  "s_waitcnt vmcnt(2)\n" \
  "v_mfma_f32_16x16x32_bf16 v[140:143], %[selr], v[132:135], v[140:143]\n" \
  "s_waitcnt vmcnt(1)\n" \
  "v_mfma_f32_16x16x32_bf16 v[140:143], %[selr], v[136:139], v[140:143]\n" \
  "s_nop 7\n" \
  "v_cmp_u_f32 vcc, v140, v141\n" \
  "s_cbranch_vccnz sr_%=\n" \
  "rok_%=:\n" \
  "v_add_f32_dpp v140, v140, v140 row_ror:8 row_mask:0xf bank_mask:0xf\n" \
  "v_add_f32_dpp v141, v141, v141 row_ror:8 row_mask:0xf bank_mask:0xf\n" \
  "v_add_f32_dpp v142, v142, v142 row_ror:8 row_mask:0xf bank_mask:0xf\n" \
  "v_add_f32_dpp v143, v143, v143 row_ror:8 row_mask:0xf bank_mask:0xf\n" \
  "v_cvt_pk_bf16_f32 v144, v140, v141\n" \
  "v_cvt_pk_bf16_f32 v145, v142, v143\n" \
  "s_nop 0\n" \
  "v_mov_b32_dpp v146, v144 quad_perm:[1,0,3,2] row_mask:0xf bank_mask:0xf\n" \
  "v_mov_b32_dpp v147, v145 quad_perm:[1,0,3,2] row_mask:0xf bank_mask:0xf\n" \
  "s_mov_b64 exec, %[mgs]\n" \
  "buffer_store_dwordx4 v[144:147], v152, %[rs], s86 offen sc1\n" \
  "s_mov_b64 exec, -1\n" \
  "v_mov_b64 v[148:149], -1\n" \
  "v_mov_b64 v[150:151], -1\n" \
  "buffer_store_dwordx4 v[148:151], %[l16], %[rs], s85 offen sc1\n" \
  "buffer_store_dwordx4 v[148:151], %[l16], %[rs], s85 offen offset:1024 sc1\n" \
  "buffer_store_dwordx4 v[148:151], %[l16], %[rs], s85 offen offset:2048 sc1\n" \
  "buffer_store_dwordx4 v[148:151], %[l16], %[rs], s85 offen offset:3072 sc1\n" \
  "s_waitcnt vmcnt(5)\n" \
  "endb_%=:\n" \
  "s_barrier\n" \
  "s_add_u32 s68, s64, 1\n" \
  "v_mov_b32 v90, s68\n" \
  "s_mov_b64 exec, %[mfl]\n" \
  "buffer_store_dword v90, off, %[rs], s59 sc1\n" \
  "s_mov_b64 exec, -1\n" \
  "s_add_u32 s64, s64, 1\n" \
  "s_add_u32 s74, s74, 16384\n" \
  "s_add_u32 s75, s75, 16384\n" \
  "s_add_u32 s86, s86, 16384\n" \
  "s_add_u32 s87, s87, 128\n" \
  "s_add_u32 s83, s83, %[kms]\n" \
  "s_branch step_%=\n" \
  "last_%=:\n" \
  "s_waitcnt lgkmcnt(0)\n" \
  "v_smfmac_f32_16x16x64_bf16 v[204:207], v[112:115], v[140:147], %[idx]\n" \
  "v_smfmac_f32_16x16x64_bf16 v[208:211], v[112:115], v[148:155], %[idx]\n" \
  "v_smfmac_f32_16x16x64_bf16 v[204:207], v[116:119], v[156:163], %[idx]\n" \
  "v_smfmac_f32_16x16x64_bf16 v[208:211], v[116:119], v[164:171], %[idx]\n" \
  "v_smfmac_f32_16x16x64_bf16 v[204:207], v[120:123], v[172:179], %[idx]\n" \
  "v_smfmac_f32_16x16x64_bf16 v[208:211], v[120:123], v[180:187], %[idx]\n" \
  "s_nop 7\n" \
  "v_pk_add_f32 v[204:205], v[204:205], v[206:207]\n" \
  "v_pk_add_f32 v[208:209], v[208:209], v[210:211]\n" \
  "v_cvt_pk_bf16_f32 v240, v204, v205\n" \
  "v_cvt_pk_bf16_f32 v241, v208, v209\n" \
  "s_bitcmp1_b32 %[role], 0\n" \
  "s_cbranch_scc1 pv1_%=\n" \
  "buffer_store_dwordx4 v[236:239], %[l16], %[rs], s83 offen sc1\n" \
  "s_branch pd1_%=\n" \
  "pv1_%=:\n" \
  "v_lshrrev_b32 v90, 1, %[l16]\n" \
  "buffer_store_dwordx2 v[240:241], v90, %[rs], s83 offen sc1\n" \
  "pd1_%=:\n" \
  "s_waitcnt vmcnt(0)\n" \
  "s_barrier\n" \
  "s_bitcmp1_b32 %[role], 3\n" \
  "s_cbranch_scc0 fin_%=\n" \
  "v_mov_b32 v90, 64\n" \
  "s_mov_b64 exec, 1\n" \
  "buffer_store_dword v90, off, %[rs], s59 sc1\n" \
  "s_mov_b64 exec, -1\n" \
  "s_branch fin_%=\n" \
  "kv_%=:\n" \
  "s_waitcnt vmcnt(0)\n" \
  "s_mov_b32 s72, 0\n" \
  "s_add_u32 s70, %[flq], 0\n" \
  "kvr_%=:\n" \
  "s_cmp_lg_u32 s73, 0\n" \
  "s_cbranch_scc1 kvok_%=\n" \
  "buffer_load_dword v90, off, %[rs], s70 sc1\n" \
  "s_waitcnt vmcnt(0)\n" \
  "v_readfirstlane_b32 s71, v90\n" \
  "s_nop 1\n" \
  "s_cmp_gt_u32 s71, s69\n" \
  "s_cbranch_scc1 kvok_%=\n" \
  "s_add_u32 s72, s72, 1\n" \
  "s_cmp_gt_u32 s72, %[cap]\n" \
  "s_cbranch_scc1 kvx_%=\n" \
  "s_and_b32 s68, s72, 255\n" \
  "s_cbranch_scc1 kvr_%=\n" \
  "buffer_load_dword v91, off, %[rs], s77 sc1\n" \
  "s_waitcnt vmcnt(0)\n" \
  "v_readfirstlane_b32 s68, v91\n" \
  "s_nop 1\n" \
  "s_cmp_eq_u32 s68, 0\n" \
  "s_cbranch_scc1 kvr_%=\n" \
  "kvx_%=:\n" \
  "s_add_u32 s68, s64, 256\n" \
  "v_mov_b32 v91, s68\n" \
  "s_mov_b64 exec, 1\n" \
  "buffer_store_dword v91, off, %[rs], s77 sc1\n" \
  "s_mov_b64 exec, -1\n" \
  "s_waitcnt vmcnt(0)\n" \
  "s_mov_b32 s73, 1\n" \
  "s_branch kvok_%=\n" \
  "sg0_%=:\n" \
  "s_waitcnt vmcnt(0)\n" \
  "s_mov_b32 s72, 0\n" \
  "s_mov_b32 s57, %[dx]\n" \
  "sg0r_%=:\n" \
  "s_cmp_lg_u32 s73, 0\n" \
  "s_cbranch_scc1 g0ok_%=\n" \
  "buffer_load_dwordx4 v[92:95], %[vg], %[rs], s74 offen sc1\n" \
  "s_waitcnt vmcnt(0)\n" \
  "v_max3_u32 v90, v92, v93, v94\n" \
  "v_max_u32 v90, v90, v95\n" \
  "v_cmp_eq_u32 vcc, -1, v90\n" \
  "s_cbranch_vccz g0ok_%=\n" \
  "s_add_u32 s72, s72, 1\n" \
  "s_cmp_gt_u32 s72, %[cap]\n" \
  "s_cbranch_scc1 sg0x_%=\n" \
  "s_and_b32 s68, s72, 255\n" \
  "s_cbranch_scc1 sg0r_%=\n" \
  "buffer_load_dword v91, off, %[rs], s77 sc1\n" \
  "s_waitcnt vmcnt(0)\n" \
  "v_readfirstlane_b32 s68, v91\n" \
  "s_nop 1\n" \
  "s_cmp_eq_u32 s68, 0\n" \
  "s_cbranch_scc1 sg0r_%=\n" \
  "sg0x_%=:\n" \
  "s_add_u32 s68, s64, 512\n" \
  "v_mov_b32 v91, s68\n" \
  "s_mov_b64 exec, 1\n" \
  "buffer_store_dword v91, off, %[rs], s77 sc1\n" \
  "s_mov_b64 exec, -1\n" \
  "s_waitcnt vmcnt(0)\n" \
  "s_mov_b32 s73, 1\n" \
  "s_branch g0ok_%=\n" \
  "sg1_%=:\n" \
  "s_waitcnt vmcnt(0)\n" \
  "s_mov_b32 s72, 0\n" \
  "s_mov_b32 s57, %[dx]\n" \
  "sg1r_%=:\n" \
  "s_cmp_lg_u32 s73, 0\n" \
  "s_cbranch_scc1 g1ok_%=\n" \
  "buffer_load_dwordx4 v[96:99], %[vg], %[rs], s75 offen sc1\n" \
  "s_waitcnt vmcnt(0)\n" \
  "v_max3_u32 v90, v96, v97, v98\n" \
  "v_max_u32 v90, v90, v99\n" \
  "v_cmp_eq_u32 vcc, -1, v90\n" \
  "s_cbranch_vccz g1ok_%=\n" \
  "s_add_u32 s72, s72, 1\n" \
  "s_cmp_gt_u32 s72, %[cap]\n" \
  "s_cbranch_scc1 sg1x_%=\n" \
  "s_and_b32 s68, s72, 255\n" \
  "s_cbranch_scc1 sg1r_%=\n" \
  "buffer_load_dword v91, off, %[rs], s77 sc1\n" \
  "s_waitcnt vmcnt(0)\n" \
  "v_readfirstlane_b32 s68, v91\n" \
  "s_nop 1\n" \
  "s_cmp_eq_u32 s68, 0\n" \
  "s_cbranch_scc1 sg1r_%=\n" \
  "sg1x_%=:\n" \
  "s_add_u32 s68, s64, 640\n" \
  "v_mov_b32 v91, s68\n" \
  "s_mov_b64 exec, 1\n" \
  "buffer_store_dword v91, off, %[rs], s77 sc1\n" \
  "s_mov_b64 exec, -1\n" \
  "s_waitcnt vmcnt(0)\n" \
  "s_mov_b32 s73, 1\n" \
  "s_branch g1ok_%=\n" \
  "vv_%=:\n" \
  "s_waitcnt vmcnt(0)\n" \
  "s_mov_b32 s72, 0\n" \
  "s_add_u32 s70, %[flq], 4096\n" \
  "vvr_%=:\n" \
  "s_cmp_lg_u32 s73, 0\n" \
  "s_cbranch_scc1 vvok_%=\n" \
  "buffer_load_dword v90, off, %[rs], s70 sc1\n" \
  "s_waitcnt vmcnt(0)\n" \
  "v_readfirstlane_b32 s58, v90\n" \
  "s_nop 1\n" \
  "s_cmp_gt_u32 s58, s69\n" \
  "s_cbranch_scc1 vvok_%=\n" \
  "s_add_u32 s72, s72, 1\n" \
  "s_cmp_gt_u32 s72, %[cap]\n" \
  "s_cbranch_scc1 vvx_%=\n" \
  "s_and_b32 s68, s72, 255\n" \
  "s_cbranch_scc1 vvr_%=\n" \
  "buffer_load_dword v91, off, %[rs], s77 sc1\n" \
  "s_waitcnt vmcnt(0)\n" \
  "v_readfirstlane_b32 s68, v91\n" \
  "s_nop 1\n" \
  "s_cmp_eq_u32 s68, 0\n" \
  "s_cbranch_scc1 vvr_%=\n" \
  "vvx_%=:\n" \
  "s_add_u32 s68, s64, 384\n" \
  "v_mov_b32 v91, s68\n" \
  "s_mov_b64 exec, 1\n" \
  "buffer_store_dword v91, off, %[rs], s77 sc1\n" \
  "s_mov_b64 exec, -1\n" \
  "s_waitcnt vmcnt(0)\n" \
  "s_mov_b32 s73, 1\n" \
  "s_branch vvok_%=\n" \
  "sst_%=:\n" \
  "s_mov_b32 s72, 0\n" \
  "sstr_%=:\n" \
  "s_cmp_lg_u32 s73, 0\n" \
  "s_cbranch_scc1 sstd_%=\n" \
  "buffer_load_dword v91, off, %[rs], s87 sc1\n" \
  "s_waitcnt vmcnt(0)\n" \
  "v_readfirstlane_b32 s76, v91\n" \
  "s_nop 1\n" \
  "s_cmp_eq_u32 s76, -1\n" \
  "s_cbranch_scc0 timed_%=\n" \
  "s_add_u32 s72, s72, 1\n" \
  "s_cmp_gt_u32 s72, %[cap]\n" \
  "s_cbranch_scc1 sstx_%=\n" \
  "s_and_b32 s68, s72, 255\n" \
  "s_cbranch_scc1 sstr_%=\n" \
  "buffer_load_dword v91, off, %[rs], s77 sc1\n" \
  "s_waitcnt vmcnt(0)\n" \
  "v_readfirstlane_b32 s68, v91\n" \
  "s_nop 1\n" \
  "s_cmp_eq_u32 s68, 0\n" \
  "s_cbranch_scc1 sstr_%=\n" \
  "sstx_%=:\n" \
  "s_add_u32 s68, s64, 896\n" \
  "v_mov_b32 v91, s68\n" \
  "s_mov_b64 exec, 1\n" \
  "buffer_store_dword v91, off, %[rs], s77 sc1\n" \
  "s_mov_b64 exec, -1\n" \
  "s_waitcnt vmcnt(0)\n" \
  "s_mov_b32 s73, 1\n" \
  "sstd_%=:\n" \
  "s_memrealtime s[66:67]\n" \
  "s_waitcnt lgkmcnt(0)\n" \
  "s_mov_b32 s76, s66\n" \
  "s_branch timed_%=\n" \
  "sr_%=:\n" \
  "s_mov_b32 s72, 0\n" \
  "srr_%=:\n" \
  "s_cmp_lg_u32 s73, 0\n" \
  "s_cbranch_scc1 srd_%=\n" \
  "v_max3_u32 v90, v124, v125, v126\n" \
  "v_max3_u32 v90, v90, v127, v128\n" \
  "v_max3_u32 v90, v90, v129, v130\n" \
  "v_max3_u32 v90, v90, v131, v132\n" \
  "v_max3_u32 v90, v90, v133, v134\n" \
  "v_max3_u32 v90, v90, v135, v136\n" \
  "v_max3_u32 v90, v90, v137, v138\n" \
  "v_max_u32 v90, v90, v139\n" \
  "v_cmp_eq_u32 vcc, -1, v90\n" \
  "s_cbranch_vccz srd_%=\n" \
  "buffer_load_dwordx4 v[124:127], %[l16], %[rs], s85 offen offset:0 sc1\n" \
  "buffer_load_dwordx4 v[128:131], %[l16], %[rs], s85 offen offset:1024 sc1\n" \
  "buffer_load_dwordx4 v[132:135], %[l16], %[rs], s85 offen offset:2048 sc1\n" \
  "buffer_load_dwordx4 v[136:139], %[l16], %[rs], s85 offen offset:3072 sc1\n" \
  "s_waitcnt vmcnt(0)\n" \
  "s_add_u32 s72, s72, 1\n" \
  "s_cmp_gt_u32 s72, %[cap]\n" \
  "s_cbranch_scc1 srx_%=\n" \
  "s_and_b32 s68, s72, 255\n" \
  "s_cbranch_scc1 srr_%=\n" \
  "buffer_load_dword v91, off, %[rs], s77 sc1\n" \
  "s_waitcnt vmcnt(0)\n" \
  "v_readfirstlane_b32 s68, v91\n" \
  "s_nop 1\n" \
  "s_cmp_eq_u32 s68, 0\n" \
  "s_cbranch_scc1 srr_%=\n" \
  "srx_%=:\n" \
  "s_add_u32 s68, s64, 768\n" \
  "v_mov_b32 v91, s68\n" \
  "s_mov_b64 exec, 1\n" \
  "buffer_store_dword v91, off, %[rs], s77 sc1\n" \
  "s_mov_b64 exec, -1\n" \
  "s_waitcnt vmcnt(0)\n" \
  "s_mov_b32 s73, 1\n" \
  "srd_%=:\n" \
  "s_nop 7\n" \
  "v_mfma_f32_16x16x32_bf16 v[140:143], %[selr], v[124:127], 0\n" \
  "v_mfma_f32_16x16x32_bf16 v[140:143], %[selr], v[128:131], v[140:143]\n" \
  "v_mfma_f32_16x16x32_bf16 v[140:143], %[selr], v[132:135], v[140:143]\n" \
  "v_mfma_f32_16x16x32_bf16 v[140:143], %[selr], v[136:139], v[140:143]\n" \
  "s_nop 7\n" \
  "s_branch rok_%=\n" \
  "atab_%=:\n" \
  "ds_read_b64_tr_b16 a[0:1], v110 offset:0\n" \
  "ds_read_b64_tr_b16 a[2:3], v111 offset:2048\n" \
  "ds_read_b64_tr_b16 a[4:5], v110 offset:8192\n" \
  "ds_read_b64_tr_b16 a[6:7], v111 offset:10240\n" \
  "ds_read_b64_tr_b16 a[64:65], v110 offset:8\n" \
  "ds_read_b64_tr_b16 a[66:67], v111 offset:2056\n" \
  "ds_read_b64_tr_b16 a[68:69], v110 offset:8200\n" \
  "ds_read_b64_tr_b16 a[70:71], v111 offset:10248\n" \
  "s_setpc_b64 s[86:87]\n" \
  "s_nop 0\n" \
  "ds_read_b64_tr_b16 a[8:9], v110 offset:0\n" \
  "ds_read_b64_tr_b16 a[10:11], v111 offset:2048\n" \
  "ds_read_b64_tr_b16 a[12:13], v110 offset:8192\n" \
  "ds_read_b64_tr_b16 a[14:15], v111 offset:10240\n" \
  "ds_read_b64_tr_b16 a[72:73], v110 offset:8\n" \
  "ds_read_b64_tr_b16 a[74:75], v111 offset:2056\n" \
  "ds_read_b64_tr_b16 a[76:77], v110 offset:8200\n" \
  "ds_read_b64_tr_b16 a[78:79], v111 offset:10248\n" \
  "s_setpc_b64 s[86:87]\n" \
  "s_nop 0\n" \
  "ds_read_b64_tr_b16 a[16:17], v110 offset:0\n" \
  "ds_read_b64_tr_b16 a[18:19], v111 offset:2048\n" \
  "ds_read_b64_tr_b16 a[20:21], v110 offset:8192\n" \
  "ds_read_b64_tr_b16 a[22:23], v111 offset:10240\n" \
  "ds_read_b64_tr_b16 a[80:81], v110 offset:8\n" \
  "ds_read_b64_tr_b16 a[82:83], v111 offset:2056\n" \
  "ds_read_b64_tr_b16 a[84:85], v110 offset:8200\n" \
  "ds_read_b64_tr_b16 a[86:87], v111 offset:10248\n" \
  "s_setpc_b64 s[86:87]\n" \
  "s_nop 0\n" \
  "ds_read_b64_tr_b16 a[24:25], v110 offset:0\n" \
  "ds_read_b64_tr_b16 a[26:27], v111 offset:2048\n" \
  "ds_read_b64_tr_b16 a[28:29], v110 offset:8192\n" \
  "ds_read_b64_tr_b16 a[30:31], v111 offset:10240\n" \
  "ds_read_b64_tr_b16 a[88:89], v110 offset:8\n" \
  "ds_read_b64_tr_b16 a[90:91], v111 offset:2056\n" \
  "ds_read_b64_tr_b16 a[92:93], v110 offset:8200\n" \
  "ds_read_b64_tr_b16 a[94:95], v111 offset:10248\n" \
  "s_setpc_b64 s[86:87]\n" \
  "s_nop 0\n" \
  "ds_read_b64_tr_b16 a[32:33], v110 offset:0\n" \
  "ds_read_b64_tr_b16 a[34:35], v111 offset:2048\n" \
  "ds_read_b64_tr_b16 a[36:37], v110 offset:8192\n" \
  "ds_read_b64_tr_b16 a[38:39], v111 offset:10240\n" \
  "ds_read_b64_tr_b16 a[96:97], v110 offset:8\n" \
  "ds_read_b64_tr_b16 a[98:99], v111 offset:2056\n" \
  "ds_read_b64_tr_b16 a[100:101], v110 offset:8200\n" \
  "ds_read_b64_tr_b16 a[102:103], v111 offset:10248\n" \
  "s_setpc_b64 s[86:87]\n" \
  "s_nop 0\n" \
  "ds_read_b64_tr_b16 a[40:41], v110 offset:0\n" \
  "ds_read_b64_tr_b16 a[42:43], v111 offset:2048\n" \
  "ds_read_b64_tr_b16 a[44:45], v110 offset:8192\n" \
  "ds_read_b64_tr_b16 a[46:47], v111 offset:10240\n" \
  "ds_read_b64_tr_b16 a[104:105], v110 offset:8\n" \
  "ds_read_b64_tr_b16 a[106:107], v111 offset:2056\n" \
  "ds_read_b64_tr_b16 a[108:109], v110 offset:8200\n" \
  "ds_read_b64_tr_b16 a[110:111], v111 offset:10248\n" \
  "s_setpc_b64 s[86:87]\n" \
  "s_nop 0\n" \
  "ds_read_b64_tr_b16 a[48:49], v110 offset:0\n" \
  "ds_read_b64_tr_b16 a[50:51], v111 offset:2048\n" \
  "ds_read_b64_tr_b16 a[52:53], v110 offset:8192\n" \
  "ds_read_b64_tr_b16 a[54:55], v111 offset:10240\n" \
  "ds_read_b64_tr_b16 a[112:113], v110 offset:8\n" \
  "ds_read_b64_tr_b16 a[114:115], v111 offset:2056\n" \
  "ds_read_b64_tr_b16 a[116:117], v110 offset:8200\n" \
  "ds_read_b64_tr_b16 a[118:119], v111 offset:10248\n" \
  "s_setpc_b64 s[86:87]\n" \
  "s_nop 0\n" \
  "ds_read_b64_tr_b16 a[56:57], v110 offset:0\n" \
  "ds_read_b64_tr_b16 a[58:59], v111 offset:2048\n" \
  "ds_read_b64_tr_b16 a[60:61], v110 offset:8192\n" \
  "ds_read_b64_tr_b16 a[62:63], v111 offset:10240\n" \
  "ds_read_b64_tr_b16 a[120:121], v110 offset:8\n" \
  "ds_read_b64_tr_b16 a[122:123], v111 offset:2056\n" \
  "ds_read_b64_tr_b16 a[124:125], v110 offset:8200\n" \
  "ds_read_b64_tr_b16 a[126:127], v111 offset:10248\n" \
  "s_setpc_b64 s[86:87]\n" \
  "s_nop 0\n" \
  "ds_read_b64_tr_b16 a[128:129], v110 offset:0\n" \
  "ds_read_b64_tr_b16 a[130:131], v111 offset:2048\n" \
  "ds_read_b64_tr_b16 a[132:133], v110 offset:8192\n" \
  "ds_read_b64_tr_b16 a[134:135], v111 offset:10240\n" \
  "ds_read_b64_tr_b16 a[192:193], v110 offset:8\n" \
  "ds_read_b64_tr_b16 a[194:195], v111 offset:2056\n" \
  "ds_read_b64_tr_b16 a[196:197], v110 offset:8200\n" \
  "ds_read_b64_tr_b16 a[198:199], v111 offset:10248\n" \
  "s_setpc_b64 s[86:87]\n" \
  "s_nop 0\n" \
  "ds_read_b64_tr_b16 a[136:137], v110 offset:0\n" \
  "ds_read_b64_tr_b16 a[138:139], v111 offset:2048\n" \
  "ds_read_b64_tr_b16 a[140:141], v110 offset:8192\n" \
  "ds_read_b64_tr_b16 a[142:143], v111 offset:10240\n" \
  "ds_read_b64_tr_b16 a[200:201], v110 offset:8\n" \
  "ds_read_b64_tr_b16 a[202:203], v111 offset:2056\n" \
  "ds_read_b64_tr_b16 a[204:205], v110 offset:8200\n" \
  "ds_read_b64_tr_b16 a[206:207], v111 offset:10248\n" \
  "s_setpc_b64 s[86:87]\n" \
  "s_nop 0\n" \
  "ds_read_b64_tr_b16 a[144:145], v110 offset:0\n" \
  "ds_read_b64_tr_b16 a[146:147], v111 offset:2048\n" \
  "ds_read_b64_tr_b16 a[148:149], v110 offset:8192\n" \
  "ds_read_b64_tr_b16 a[150:151], v111 offset:10240\n" \
  "ds_read_b64_tr_b16 a[208:209], v110 offset:8\n" \
  "ds_read_b64_tr_b16 a[210:211], v111 offset:2056\n" \
  "ds_read_b64_tr_b16 a[212:213], v110 offset:8200\n" \
  "ds_read_b64_tr_b16 a[214:215], v111 offset:10248\n" \
  "s_setpc_b64 s[86:87]\n" \
  "s_nop 0\n" \
  "ds_read_b64_tr_b16 a[152:153], v110 offset:0\n" \
  "ds_read_b64_tr_b16 a[154:155], v111 offset:2048\n" \
  "ds_read_b64_tr_b16 a[156:157], v110 offset:8192\n" \
  "ds_read_b64_tr_b16 a[158:159], v111 offset:10240\n" \
  "ds_read_b64_tr_b16 a[216:217], v110 offset:8\n" \
  "ds_read_b64_tr_b16 a[218:219], v111 offset:2056\n" \
  "ds_read_b64_tr_b16 a[220:221], v110 offset:8200\n" \
  "ds_read_b64_tr_b16 a[222:223], v111 offset:10248\n" \
  "s_setpc_b64 s[86:87]\n" \
  "s_nop 0\n" \
  "ds_read_b64_tr_b16 a[160:161], v110 offset:0\n" \
  "ds_read_b64_tr_b16 a[162:163], v111 offset:2048\n" \
  "ds_read_b64_tr_b16 a[164:165], v110 offset:8192\n" \
  "ds_read_b64_tr_b16 a[166:167], v111 offset:10240\n" \
  "ds_read_b64_tr_b16 a[224:225], v110 offset:8\n" \
  "ds_read_b64_tr_b16 a[226:227], v111 offset:2056\n" \
  "ds_read_b64_tr_b16 a[228:229], v110 offset:8200\n" \
  "ds_read_b64_tr_b16 a[230:231], v111 offset:10248\n" \
  "s_setpc_b64 s[86:87]\n" \
  "s_nop 0\n" \
  "ds_read_b64_tr_b16 a[168:169], v110 offset:0\n" \
  "ds_read_b64_tr_b16 a[170:171], v111 offset:2048\n" \
  "ds_read_b64_tr_b16 a[172:173], v110 offset:8192\n" \
  "ds_read_b64_tr_b16 a[174:175], v111 offset:10240\n" \
  "ds_read_b64_tr_b16 a[232:233], v110 offset:8\n" \
  "ds_read_b64_tr_b16 a[234:235], v111 offset:2056\n" \
  "ds_read_b64_tr_b16 a[236:237], v110 offset:8200\n" \
  "ds_read_b64_tr_b16 a[238:239], v111 offset:10248\n" \
  "s_setpc_b64 s[86:87]\n" \
  "s_nop 0\n" \
  "ds_read_b64_tr_b16 a[176:177], v110 offset:0\n" \
  "ds_read_b64_tr_b16 a[178:179], v111 offset:2048\n" \
  "ds_read_b64_tr_b16 a[180:181], v110 offset:8192\n" \
  "ds_read_b64_tr_b16 a[182:183], v111 offset:10240\n" \
  "ds_read_b64_tr_b16 a[240:241], v110 offset:8\n" \
  "ds_read_b64_tr_b16 a[242:243], v111 offset:2056\n" \
  "ds_read_b64_tr_b16 a[244:245], v110 offset:8200\n" \
  "ds_read_b64_tr_b16 a[246:247], v111 offset:10248\n" \
  "s_setpc_b64 s[86:87]\n" \
  "s_nop 0\n" \
  "ds_read_b64_tr_b16 a[184:185], v110 offset:0\n" \
  "ds_read_b64_tr_b16 a[186:187], v111 offset:2048\n" \
  "ds_read_b64_tr_b16 a[188:189], v110 offset:8192\n" \
  "ds_read_b64_tr_b16 a[190:191], v111 offset:10240\n" \
  "ds_read_b64_tr_b16 a[248:249], v110 offset:8\n" \
  "ds_read_b64_tr_b16 a[250:251], v111 offset:2056\n" \
  "ds_read_b64_tr_b16 a[252:253], v110 offset:8200\n" \
  "ds_read_b64_tr_b16 a[254:255], v111 offset:10248\n" \
  "s_setpc_b64 s[86:87]\n" \
  "s_nop 0\n" \
  "fin_%=:\n" \
  "s_waitcnt vmcnt(0) lgkmcnt(0)\n" \
  ""
#define CHAIN_CLOBBERS \
  "v0", "v1", "v2", "v3", "v4", "v5", "v6", "v7", "v8", "v9", "v10", "v11", "v12", "v13", "v14", "v15", "v16", "v17", "v18", "v19", "v20", "v21", "v22", \
  "v23", "v24", "v25", "v26", "v27", "v28", "v29", "v30", "v31", "v32", "v33", "v34", "v35", "v36", "v37", "v38", "v39", "v40", "v41", "v42", "v43", \
  "v44", "v45", "v46", "v47", "v48", "v49", "v50", "v51", "v52", "v53", "v54", "v55", "v56", "v57", "v58", "v59", "v60", "v61", "v62", "v63", "v90", \
  "v91", "v92", "v93", "v94", "v95", "v96", "v97", "v98", "v99", "v100", "v101", "v102", "v103", "v104", "v105", "v106", "v107", "v108", "v109", "v110", \
  "v111", "v112", "v113", "v114", "v115", "v116", "v117", "v118", "v119", "v120", "v121", "v122", "v123", "v124", "v125", "v126", "v127", "v128", \
  "v129", "v130", "v131", "v132", "v133", "v134", "v135", "v136", "v137", "v138", "v139", "v140", "v141", "v142", "v143", "v144", "v145", "v146", \
  "v147", "v148", "v149", "v150", "v151", "v152", "v153", "v154", "v155", "v156", "v157", "v158", "v159", "v160", "v161", "v162", "v163", "v164", \
  "v165", "v166", "v167", "v168", "v169", "v170", "v171", "v172", "v173", "v174", "v175", "v176", "v177", "v178", "v179", "v180", "v181", "v182", \
  "v183", "v184", "v185", "v186", "v187", "v188", "v189", "v190", "v191", "v192", "v193", "v194", "v195", "v196", "v197", "v198", "v199", "v200", \
  "v201", "v202", "v203", "v204", "v205", "v206", "v207", "v208", "v209", "v210", "v211", "v212", "v213", "v214", "v215", "v216", "v217", "v218", \
  "v219", "v220", "v221", "v222", "v223", "v224", "v225", "v226", "v227", "v228", "v229", "v230", "v231", "v232", "v233", "v234", "v235", "v236", \
  "v237", "v238", "v239", "v251", "a0", "a1", "a2", "a3", "a4", "a5", "a6", "a7", "a8", "a9", "a10", "a11", "a12", "a13", "a14", "a15", "a16", "a17", \
  "a18", "a19", "a20", "a21", "a22", "a23", "a24", "a25", "a26", "a27", "a28", "a29", "a30", "a31", "a32", "a33", "a34", "a35", "a36", "a37", "a38", \
  "a39", "a40", "a41", "a42", "a43", "a44", "a45", "a46", "a47", "a48", "a49", "a50", "a51", "a52", "a53", "a54", "a55", "a56", "a57", "a58", "a59", \
  "a60", "a61", "a62", "a63", "a64", "a65", "a66", "a67", "a68", "a69", "a70", "a71", "a72", "a73", "a74", "a75", "a76", "a77", "a78", "a79", "a80", \
  "a81", "a82", "a83", "a84", "a85", "a86", "a87", "a88", "a89", "a90", "a91", "a92", "a93", "a94", "a95", "a96", "a97", "a98", "a99", "a100", "a101", \
  "a102", "a103", "a104", "a105", "a106", "a107", "a108", "a109", "a110", "a111", "a112", "a113", "a114", "a115", "a116", "a117", "a118", "a119", \
  "a120", "a121", "a122", "a123", "a124", "a125", "a126", "a127", "a128", "a129", "a130", "a131", "a132", "a133", "a134", "a135", "a136", "a137", \
  "a138", "a139", "a140", "a141", "a142", "a143", "a144", "a145", "a146", "a147", "a148", "a149", "a150", "a151", "a152", "a153", "a154", "a155", \
  "a156", "a157", "a158", "a159", "a160", "a161", "a162", "a163", "a164", "a165", "a166", "a167", "a168", "a169", "a170", "a171", "a172", "a173", \
  "a174", "a175", "a176", "a177", "a178", "a179", "a180", "a181", "a182", "a183", "a184", "a185", "a186", "a187", "a188", "a189", "a190", "a191", \
  "a192", "a193", "a194", "a195", "a196", "a197", "a198", "a199", "a200", "a201", "a202", "a203", "a204", "a205", "a206", "a207", "a208", "a209", \
  "a210", "a211", "a212", "a213", "a214", "a215", "a216", "a217", "a218", "a219", "a220", "a221", "a222", "a223", "a224", "a225", "a226", "a227", \
  "a228", "a229", "a230", "a231", "a232", "a233", "a234", "a235", "a236", "a237", "a238", "a239", "a240", "a241", "a242", "a243", "a244", "a245", \
  "a246", "a247", "a248", "a249", "a250", "a251", "a252", "a253", "a254", "a255", "s57", "s58", "s59", "s60", "s61", "s62", "s63", "s64", "s65", "s66", \
  "s67", "s68", "s69", "s70", "s71", "s72", "s73", "s74", "s75", "s76", "s77", "s78", "s79", "s80", "s81", "s82", "s83", "s84", "s85", "s86", "s87", \
  "s88", "s89", "s90", "s91", "s92"
constexpr int CH_GX = 128 * 1024, CH_SC = CH_GX + 8192, CH_SM = CH_SC + 8192, CH_OA = CH_SC + 12288;

DEV void role_chain(const Params& p, int c, lptr L) {
  const int tid = threadIdx.x, lane = tid & 63, w = __builtin_amdgcn_readfirstlane(tid >> 6);
  const int h = c >> 1, ch = c & 1;
  const int n = lane & 15, g = lane >> 4, q4 = (lane >> 2) & 3, pp = lane & 3;
  const unsigned Lb = (unsigned)(size_t)L;
  auto U = [](int v) { return (unsigned)__builtin_amdgcn_readfirstlane(v); };

#define T_LAG 0
  if (T_LAG && ch) hold_back(rtc(), T_LAG);
  unsigned ps8[4], ps1[4];
#pragma unroll
  for (int q = 0; q < 4; ++q) {
    const unsigned row = 16 * w + 4 * q + g, sw = ((n ^ swz(row)) << 4);
    ps8[q] = row * 8192 + sw; ps1[q] = row * 1024 + sw;
  }
  auto tradr = [&](unsigned row, unsigned c4) { return Lb + 256 * row + 16 * ((c4 >> 1) ^ swz(row)) + 8 * (c4 & 1); };
  const unsigned rdl = 16 * (g >> 1) + 4 * (q4 & 1) + 2 * (q4 >> 1) + (g & 1), rel = 16 * (g >> 1) + 8 * (q4 >> 1) + 4 * (g & 1) + (q4 & 1);
  unsigned paq0 = tradr(rdl, 8 * w + 2 * pp), paq1 = tradr(rdl + 8, 8 * w + 2 * pp) - 2048;
  unsigned pav = tradr(rdl, 8 * w + pp);
  unsigned pao[4];
#pragma unroll
  for (int k = 0; k < 4; ++k) pao[k] = tradr(rel + 2 * (k >> 1), 16 * (w & 1) + 4 * pp + 2 * (k & 1)) - 512 * (k >> 1);
  unsigned sigv = 0;
#pragma unroll
  for (int kk = 0; kk < 8; ++kk) { const int d = ((kk & 3) - w) & 3; sigv |= (unsigned)(kk < 4 ? (d ? 1 + d : 0) : (d ? 4 + d : 1)) << (3 * kk); }
  const unsigned sig = U(sigv), wv = U(w), lbw = U(Lb + w * 4096);
  const unsigned short* wqp = p.wq + h * 128;
  const unsigned short* wkp = p.wk + h * 128;
  const unsigned short* wvp = p.wv + h * 128;
  const unsigned short* wop = p.wo + (size_t)h * 128 * 512 + 256 * ch;

  const int idx = (lane & 2) ? 0xEEEE : 0x4444;
  v2u sq0, sq1, sk0, sk1; v4u selr;
  {
    const unsigned one = 0x3F80u;
    auto pick = [&](int k0, int want) { return (k0 == want ? one : 0u) | (k0 + 1 == want ? one << 16 : 0u); };
    const unsigned sq = pick(2 * g, n & 7), sk = pick(2 * g, 2 * (n >> 2) + (n & 1));
    sq0 = v2u{sq, 0u}; sq1 = v2u{0u, sq}; sk0 = v2u{sk, 0u}; sk1 = v2u{0u, sk};
    selr = v4u{pick(0, n), pick(2, n), pick(4, n), pick(6, n)};
  }
  const int gbase = OFF_G, rbase = OFF_R;
  const int jst = 32 * ch + 8 * w + (n >> 1);
  const unsigned l16 = lane * 16;
  const unsigned vg = (2 * g + ((n >> 1) & 1)) * 128 + (2 * (n >> 2) + (n & 1)) * 16;
  const unsigned rst = (jst >> 1) * 16384 + (jst & 1) * 4096 + (2 * g + (n & 1)) * 16;
  const unsigned lw = Lb + CH_GX + w * 1024 + l16;
  const unsigned la1 = Lb + CH_GX + ((w + 1) & 3) * 1024 + l16, la2 = Lb + CH_GX + ((w + 2) & 3) * 1024 + l16, la3 = Lb + CH_GX + ((w + 3) & 3) * 1024 + l16;
  const unsigned lv0 = Lb + w * 4096 + l16, lv1 = lv0 + 65536;
  const unsigned scw = Lb + CH_SC + w * 2048 + l16, scx = Lb + CH_SC + (n >> 3) * 1024 + l16;
  const unsigned smr = Lb + CH_SM + lane * 8;
  const unsigned ow = Lb + CH_OA + (w >> 1) * 1024 + (4 * ((n & 7) >> 1) + 2 * (g >> 1) + (n & 1) + 32 * (w & 1)) * 16 + (g & 1) * 8;
  const unsigned long long mlo = __builtin_amdgcn_ballot_w64(n < 8), mgs = __builtin_amdgcn_ballot_w64(n < 8 && (n & 1) == 0 && g < 2),
                           mfl = __builtin_amdgcn_ballot_w64(tid == 0);
  const uintptr_t wsa = (uintptr_t)p.ws;
  const v4u rsv = {(unsigned)wsa, (unsigned)(wsa >> 32) & 0xffffu, (unsigned)WS_END, 0x00020000u};
  const unsigned gb = U(gbase + w * 1024), gs0 = U(gbase + c * 128), stb = U(gbase + 64 * 16384);
  const unsigned kb = U(OFF_K + h * 524288 + w * 1024), vb = U(OFF_V + h * 524288 + 2 * w * 1024);
  const unsigned kmb = U(ch == 0 ? OFF_KM + h * 262144 + w * 1024 : OFF_VM + h * 131072 + w * 512), kms = U(ch == 0 ? 4096 : 2048);
  const unsigned rsb = U(rbase + h * 128), rrb = U(rbase + (c >> 1) * 16384 + (c & 1) * 4096);
  const unsigned flq = U(OFF_FLAG + (FL_QKV + h) * 128), flkm = U(OFF_KMF + c * 128);
  const unsigned role = U(ch | ((c == 0 && w == 0) ? 2 : 0) | (w == 3 ? 4 : 0) | (w == 0 ? 8 : 0) | (w << 4));
  const unsigned dbg = U(OFF_DBG2 + (c * 4 + w) * 4096), dbg2 = U(OFF_DBG2 + MB + (blockIdx.x * 128 + 66) * 4), oab = U(Lb + CH_OA);
  const float cs = 0.08838834764831845f * 1.4426950408889634f;
  asm volatile(
      CHAIN_ASM
      : "+{v240}"(ps8[0]), "+{v241}"(ps8[1]), "+{v242}"(ps8[2]), "+{v243}"(ps8[3]), "+{v244}"(ps1[0]), "+{v245}"(ps1[1]),
        "+{v246}"(ps1[2]), "+{v247}"(ps1[3]), "+{v248}"(paq0), "+{v249}"(paq1), "+{v250}"(pav), "+{v252}"(pao[0]), "+{v253}"(pao[1]),
        "+{v254}"(pao[2]), "+{v255}"(pao[3])
      : [idx] "{v64}"(idx), [l16] "{v65}"(l16), [sq0] "{v[66:67]}"(sq0), [sq1] "{v[68:69]}"(sq1), [sk0] "{v[70:71]}"(sk0),
        [sk1] "{v[72:73]}"(sk1), [selr] "{v[74:77]}"(selr), [vg] "{v78}"(vg), [rst] "{v79}"(rst), [lw] "{v80}"(lw), [la1] "{v81}"(la1),
        [la2] "{v82}"(la2), [la3] "{v83}"(la3), [lv0] "{v84}"(lv0), [lv1] "{v85}"(lv1), [scw] "{v86}"(scw), [scx] "{v87}"(scx),
        [smr] "{v88}"(smr), [ow] "{v89}"(ow), [rs] "s"(rsv), [mlo] "s"(mlo), [mgs] "s"(mgs), [mfl] "s"(mfl), [gb] "s"(gb),
        [gs0] "s"(gs0), [stb] "s"(stb), [kb] "s"(kb), [vb] "s"(vb), [kmb] "s"(kmb), [kms] "s"(kms), [rsb] "s"(rsb), [rrb] "s"(rrb),
        [flq] "s"(flq), [flkm] "s"(flkm), [role] "s"(role), [dbg] "s"(dbg), [dbg2] "s"(dbg2), [cs] "s"(cs), [sig] "s"(sig),
        [wv] "s"(wv), [lbw] "s"(lbw), [oab] "s"(oab), [wqp] "s"(wqp), [wkp] "s"(wkp), [wvp] "s"(wvp), [wop] "s"(wop),
        [abo] "i"(OFF_FLAG + FL_ABORT * 128), [paro] "s"(U(OFF_PAR)), [cap] "i"(SPIN_CAP), [d1] "s"(p.d1), [d2] "s"(p.d2), [dx] "s"(p.fb)
      : "memory", "vcc", "scc", CHAIN_CLOBBERS);
}

DEV void stage_x(const unsigned char* src, unsigned dst, int w, int lane) {
#pragma unroll
  for (int q = 0; q < 16; ++q) glds(src, (w * 16 + q) * 1024 + lane * 16, dst + (w * 16 + q) * 1024);
}

#define MFMA_WA(acc, w, x) asm volatile("v_mfma_f32_16x16x32_bf16 %0, %1, %2, %0" : "+v"(acc) : "a"(w), "v"(x))
#define MFMA_WB(acc, x, w) asm volatile("v_mfma_f32_16x16x32_bf16 %0, %1, %2, %0" : "+v"(acc) : "v"(x), "a"(w))
DEV v4u pin_acc4(v4u x) { v4u y; asm volatile("" : "=a"(y) : "0"(x)); return y; }

template <bool TRANS, bool PRE> DEV void qkv_tile(const v4u (&wf)[16][2], lptr X, rsrc_t rs, int obase, int h, int i, int w, int lane, const unsigned char* nsrc, unsigned ndst) {
  f32x4 acc[4][2];
#pragma unroll
  for (int T = 0; T < 4; ++T) { acc[T][0] = f32x4{0.f, 0.f, 0.f, 0.f}; acc[T][1] = acc[T][0]; }
  asm volatile("s_nop 1" : "+v"(acc[0][0]), "+v"(acc[0][1]), "+v"(acc[1][0]), "+v"(acc[1][1]), "+v"(acc[2][0]), "+v"(acc[2][1]), "+v"(acc[3][0]), "+v"(acc[3][1]));
  v4u xr[2][4];
#pragma unroll
  for (int T = 0; T < 4; ++T) xr[0][T] = lds16(X + (T * 16) * 1024);
  SB();
#pragma unroll
  for (int kk = 0; kk < 16; ++kk)
#pragma unroll
    for (int T = 0; T < 4; ++T) {
      if (kk + 1 < 16) xr[(kk + 1) & 1][T] = lds16(X + (T * 16 + kk + 1) * 1024);
      if (PRE && T == 1) glds(nsrc, (w * 16 + kk) * 1024 + lane * 16, ndst + (w * 16 + kk) * 1024);
      SB();
      if (TRANS) { MFMA_WA(acc[T][0], wf[kk][0], xr[kk & 1][T]); MFMA_WA(acc[T][1], wf[kk][1], xr[kk & 1][T]); }
      else { MFMA_WB(acc[T][0], xr[kk & 1][T], wf[kk][0]); MFMA_WB(acc[T][1], xr[kk & 1][T], wf[kk][1]); }
      SB();
    }
  asm volatile("s_nop 7" : "+v"(acc[0][0]), "+v"(acc[0][1]), "+v"(acc[1][0]), "+v"(acc[1][1]), "+v"(acc[2][0]), "+v"(acc[2][1]), "+v"(acc[3][0]), "+v"(acc[3][1]));
  if (TRANS) {
#pragma unroll
    for (int T = 0; T < 4; ++T) {
      const f32x4 a = acc[T][0], b = acc[T][1];
      st16(v4u{pk2(a[0], a[1]), pk2(a[2], a[3]), pk2(b[0], b[1]), pk2(b[2], b[3])}, rs, obase + qkv_block(2 * i + (T >> 1), h) + ((T & 1) * 4 + w) * 1024);
    }
  } else {
#pragma unroll
    for (int sg = 0; sg < 2; ++sg)
#pragma unroll
      for (int t = 0; t < 2; ++t) {
        const f32x4 a = acc[2 * sg][t], b = acc[2 * sg + 1][t];
        st16(v4u{pk2(a[0], a[1]), pk2(a[2], a[3]), pk2(b[0], b[1]), pk2(b[2], b[3])}, rs, obase + qkv_block(2 * i + sg, h) + (2 * w + t) * 1024);
      }
  }
}

template <bool KV> DEV void role_qkv_t(const Params& p, int m, lptr L) {
  constexpr int EARLY = 4;
  const int tid = threadIdx.x, lane = tid & 63, w = __builtin_amdgcn_readfirstlane(tid >> 6);
  const int g = lane >> 4, q = (lane >> 2) & 3, pp = lane & 3;
  const int hA = KV ? m : 2 * (m - 32), hB = KV ? m : hA + 1;
  const rsrc_t rs = make_rsrc(p.ws, WS_END);
  const unsigned Lb = (unsigned)(size_t)L;
  hold_back(rtc(), KV ? T_HOLD_KV : T_HOLD);
  v4u wa[16][2], wb[16][2];
  auto load = [&](const unsigned short* W, v4u (&wf)[16][2], bool trans) {
    sfor<0, 8>([&](auto I) { stage_chunk(W + (size_t)(64 * I) * 4096, 8192, Lb + I * 16384, w, lane); });
    WAIT_VM(0);
    BAR();
#pragma unroll
    for (int kk = 0; kk < 16; ++kk)
#pragma unroll
      for (int t = 0; t < 2; ++t) {
        const lptr slot = L + (kk >> 1) * 16384;
        const unsigned c4 = trans ? 8 * w + 2 * pp + t : 8 * w + 4 * t + pp;
        const v2u a = trfrag(slot, 32 * (kk & 1) + 8 * g + q, c4), b = trfrag(slot, 32 * (kk & 1) + 8 * g + 4 + q, c4);
        wf[kk][t] = pin_acc4(v4u{a.x, a.y, b.x, b.y});
      }
    BAR();
  };
  const unsigned char* xs = p.ws + OFF_XN;
  const int obA = (KV ? OFF_K : OFF_Q) + lane * 16, obB = (KV ? OFF_V : OFF_Q) + lane * 16;
  const int flA = OFF_FLAG + (FL_QKV + (KV ? 0 : 64) + hA) * 128, flB = OFF_FLAG + (FL_QKV + (KV ? 32 : 64) + hB) * 128;
  load((KV ? p.wk : p.wq) + hA * 128, wa, true);
  {
    const unsigned short* W = (KV ? p.wv : p.wq) + hB * 128;
    auto extract = [&](auto KK, lptr slot) {
      constexpr int kk = KK;
#pragma unroll
      for (int t = 0; t < 2; ++t) {
        const unsigned c4 = !KV ? 8 * w + 2 * pp + t : 8 * w + 4 * t + pp;
        const v2u x = trfrag(slot, 32 * (kk & 1) + 8 * g + q, c4), y = trfrag(slot, 32 * (kk & 1) + 8 * g + 4 + q, c4);
        wb[kk][t] = pin_acc4(v4u{x.x, x.y, y.x, y.y});
      }
    };
    stage_x(xs, Lb, w, lane);
    sfor<0, 6>([&](auto I) { stage_chunk(W + (size_t)(64 * I) * 4096, 8192, Lb + 65536 + I * 16384, w, lane); });
    WAIT_VM(24);
    BAR();
    qkv_tile<true, false>(wa, L + lane * 16, rs, obA, hA, 0, w, lane, nullptr, 0);
    WAIT_VM(0);
    BAR();
    if (tid == 0) st4(1u, rs, flA);
    sfor<0, 12>([&](auto K) { extract(K, L + 65536 + (K >> 1) * 16384); });
    BAR();
    sfor<6, 8>([&](auto I) { stage_chunk(W + (size_t)(64 * I) * 4096, 8192, Lb + 65536 + (I - 6) * 16384, w, lane); });
    WAIT_VM(0);
    BAR();
    sfor<12, 16>([&](auto K) { extract(K, L + 65536 + ((K >> 1) - 6) * 16384); });
    BAR();
    qkv_tile<!KV, true>(wb, L + lane * 16, rs, obB, hB, 0, w, lane, xs + 65536, Lb + 65536);
    WAIT_VM(0);
    BAR();
    if (tid == 0) st4(1u, rs, flB);
  }
  WSTAMP(1);
#pragma unroll 1
  for (int i = 1; i < 32; ++i) {
    WAIT_VM(8);
    BAR();
    WSTAMP(2 + i);
    if (i > EARLY + 1 && tid == 0) { st4((unsigned)(i - 1), rs, flA); st4((unsigned)(i - 1), rs, flB); }
    const lptr X = L + (i & 1) * 65536 + lane * 16;
    qkv_tile<true, true>(wa, X, rs, obA, hA, i, w, lane, xs + (i + 1 < 32 ? i + 1 : 31) * 65536, Lb + ((i + 1) & 1) * 65536);
    if (i <= EARLY) { WAIT_VM(0); BAR(); if (tid == 0) st4((unsigned)(i + 1), rs, flA); }
    qkv_tile<!KV, false>(wb, X, rs, obB, hB, i, w, lane, nullptr, 0);
    if (i <= EARLY) { WAIT_VM(0); BAR(); if (tid == 0) st4((unsigned)(i + 1), rs, flB); }
  }
  WAIT_VM(0);
  BAR();
  if (tid == 0) { st4(32u, rs, flA); st4(32u, rs, flB); }
}

#define MFMA_ACC(acc, a, b) MFMA_WA(acc, a, b)
DEV float gelu_tanh(float x) {
  const float c0 = -2.f * 1.4426950408889634f * 0.7978845608028654f, c1 = c0 * 0.044715f;
  const float z = x * __builtin_fmaf(x * x, c1, c0);
  return x * __builtin_amdgcn_rcpf(1.f + __builtin_amdgcn_exp2f(z));
}
DEV void role_mlp(const Params& p, int m, lptr L) {
  const int tid = threadIdx.x, lane = tid & 63, w = __builtin_amdgcn_readfirstlane(tid >> 6);
  const int j = m;
  const int g = lane >> 4, q = (lane >> 2) & 3, pp = lane & 3;
  const rsrc_t rs = make_rsrc(p.ws, WS_END);
  const unsigned Lb = (unsigned)(size_t)L;
  v4u w1f[16][2], w2f[4][2][4];
  hold_back(rtc(), T_HOLD);
  sfor<0, 8>([&](auto I) { stage_chunk(p.w1 + (size_t)(64 * I) * 1792 + 128 * j, 3584, Lb + I * 16384, w, lane); });
  WAIT_VM(0);
  BAR();
#pragma unroll
  for (int kk = 0; kk < 16; ++kk)
#pragma unroll
    for (int t = 0; t < 2; ++t) {
      const lptr slot = L + (kk >> 1) * 16384;
      const v2u a = trfrag(slot, 32 * (kk & 1) + 8 * g + q, 8 * w + 2 * pp + t), b = trfrag(slot, 32 * (kk & 1) + 8 * g + 4 + q, 8 * w + 2 * pp + t);
      w1f[kk][t] = pin_acc4(v4u{a.x, a.y, b.x, b.y});
    }
  BAR();
  sfor<0, 8>([&](auto I) { stage_chunk(p.w2 + (size_t)(128 * j + 64 * (I >> 2)) * 512 + 128 * (I & 3), 1024, Lb + I * 16384, w, lane); });
  WAIT_VM(0);
  BAR();
#pragma unroll
  for (int cg = 0; cg < 4; ++cg)
#pragma unroll
    for (int t = 0; t < 2; ++t)
#pragma unroll
      for (int kk = 0; kk < 4; ++kk) {
        const lptr slot = L + ((kk >> 1) * 4 + w) * 16384;
        const v2u a = trfrag(slot, 32 * (kk & 1) + 8 * g + q, 8 * cg + 2 * pp + t), b = trfrag(slot, 32 * (kk & 1) + 8 * g + 4 + q, 8 * cg + 2 * pp + t);
        w2f[cg][t][kk] = pin_acc4(v4u{a.x, a.y, b.x, b.y});
      }
  BAR();
  float bias[8];
  {
    const v4u bv = *(const GAS v4u*)((const GAS unsigned short*)p.b1 + 128 * j + 32 * w + 8 * g);
#pragma unroll
    for (int k = 0; k < 4; ++k) { bias[2 * k] = lo16(bv[k]); bias[2 * k + 1] = hi16(bv[k]); }
    asm volatile("s_waitcnt vmcnt(0)" : "+v"(bias[0]), "+v"(bias[1]), "+v"(bias[2]), "+v"(bias[3]), "+v"(bias[4]), "+v"(bias[5]), "+v"(bias[6]), "+v"(bias[7])::"memory");
  }
  const unsigned char* ys = p.ws + OFF_Y;
  const lptr HX = L + 131072;
  const int foff = OFF_FLAG + (FL_MLP + m) * 128;
  WSTAMP(1);
  stage_x(ys, Lb, w, lane);
#pragma unroll 1
  for (int it = 0; it < 32; ++it) {
    const int i = it;
    if (it == 0) WAIT_VM(0); else WAIT_VM(16);
    BAR();
    WSTAMP(2 + it);
    if (it >= 1 && tid == 0) st4((unsigned)(it - 1 > 0 ? it - 1 : 0), rs, foff);
    const unsigned char* nsrc = ys + (i + 1 < 32 ? i + 1 : 31) * 65536;
    const unsigned ndst = Lb + ((it + 1) & 1) * 65536;
    const lptr X = L + (it & 1) * 65536 + lane * 16;
    {
      f32x4 acc[4][2];
#pragma unroll
      for (int T = 0; T < 4; ++T) { acc[T][0] = f32x4{0.f, 0.f, 0.f, 0.f}; acc[T][1] = acc[T][0]; }
      asm volatile("s_nop 1" : "+v"(acc[0][0]), "+v"(acc[0][1]), "+v"(acc[1][0]), "+v"(acc[1][1]), "+v"(acc[2][0]), "+v"(acc[2][1]), "+v"(acc[3][0]), "+v"(acc[3][1]));
      v4u xr[2][4];
#pragma unroll
      for (int T = 0; T < 4; ++T) xr[0][T] = lds16(X + (T * 16) * 1024);
      SB();
#pragma unroll
      for (int kk = 0; kk < 16; ++kk)
#pragma unroll
        for (int T = 0; T < 4; ++T) {
          if (kk + 1 < 16) xr[(kk + 1) & 1][T] = lds16(X + (T * 16 + kk + 1) * 1024);
          if (T == 1) glds(nsrc, (w * 16 + kk) * 1024 + lane * 16, ndst + (w * 16 + kk) * 1024);
          SB();
          MFMA_ACC(acc[T][0], w1f[kk][0], xr[kk & 1][T]);
          MFMA_ACC(acc[T][1], w1f[kk][1], xr[kk & 1][T]);
          SB();
        }
      asm volatile("s_nop 7" : "+v"(acc[0][0]), "+v"(acc[0][1]), "+v"(acc[1][0]), "+v"(acc[1][1]), "+v"(acc[2][0]), "+v"(acc[2][1]), "+v"(acc[3][0]), "+v"(acc[3][1]));
#pragma unroll
      for (int T = 0; T < 4; ++T) {
        float hv[8];
#pragma unroll
        for (int r = 0; r < 4; ++r) {
          hv[r] = gelu_tanh(acc[T][0][r] + bias[r]);
          hv[4 + r] = gelu_tanh(acc[T][1][r] + bias[4 + r]);
        }
        lds16w(HX + (T * 4 + w) * 1024 + lane * 16, v4u{pk2(hv[0], hv[1]), pk2(hv[2], hv[3]), pk2(hv[4], hv[5]), pk2(hv[6], hv[7])});
      }
    }
    BAR();
#pragma unroll
    for (int tp = 0; tp < 2; ++tp) {
      f32x4 acc[2][4][2];
#pragma unroll
      for (int T = 0; T < 2; ++T)
#pragma unroll
        for (int cg = 0; cg < 4; ++cg) { acc[T][cg][0] = f32x4{0.f, 0.f, 0.f, 0.f}; acc[T][cg][1] = acc[T][cg][0]; }
#pragma unroll
      for (int T = 0; T < 2; ++T)
        asm volatile("s_nop 1" : "+v"(acc[T][0][0]), "+v"(acc[T][0][1]), "+v"(acc[T][1][0]), "+v"(acc[T][1][1]), "+v"(acc[T][2][0]), "+v"(acc[T][2][1]), "+v"(acc[T][3][0]), "+v"(acc[T][3][1]));
      v4u hf[4][2];
#pragma unroll
      for (int kk = 0; kk < 4; ++kk)
#pragma unroll
        for (int T = 0; T < 2; ++T) hf[kk][T] = lds16(HX + ((2 * tp + T) * 4 + kk) * 1024 + lane * 16);
      SB();
#pragma unroll
      for (int kk = 0; kk < 4; ++kk)
#pragma unroll
        for (int T = 0; T < 2; ++T) {
#pragma unroll
          for (int cg = 0; cg < 4; ++cg) { MFMA_ACC(acc[T][cg][0], w2f[cg][0][kk], hf[kk][T]); MFMA_ACC(acc[T][cg][1], w2f[cg][1][kk], hf[kk][T]); }
        }
#pragma unroll
      for (int T = 0; T < 2; ++T)
        asm volatile("s_nop 7" : "+v"(acc[T][0][0]), "+v"(acc[T][0][1]), "+v"(acc[T][1][0]), "+v"(acc[T][1][1]), "+v"(acc[T][2][0]), "+v"(acc[T][2][1]), "+v"(acc[T][3][0]), "+v"(acc[T][3][1]));
      const int pbase = OFF_PART + ((2 * i + tp) * NPART + 32 + j) * 32768 + lane * 16;
#pragma unroll
      for (int T = 0; T < 2; ++T)
#pragma unroll
        for (int cg = 0; cg < 4; ++cg) {
          const f32x4 a = acc[T][cg][0], b = acc[T][cg][1];
          st16(v4u{pk2(a[0], a[1]), pk2(a[2], a[3]), pk2(b[0], b[1]), pk2(b[2], b[3])}, rs, pbase + ((4 * w + cg) * 2 + T) * 1024);
        }
    }
  }
  WAIT_VM(0);
  BAR();
  if (tid == 0) st4(32u, rs, foff);
}

constexpr int FA_Q = 0, FA_K = 8192, FA_V = 16384, FA_KM = 24576, FA_VM = 28672, FA_O = 30720, FA_AV = 38912;
template <bool TAIL> DEV int next_seg(int s) { return TAIL ? s + 2 : (s == NSEG - 4 ? s + 2 : s + 1); }
template <bool TAIL> DEV void role_fatt(const Params& p, int h, lptr L0, int fl) {
  constexpr int sbeg = TAIL ? NSEG - 3 : 0;
  const int tid = threadIdx.x, lane = tid & 63, w = __builtin_amdgcn_readfirstlane(tid >> 6);
  const int n = lane & 15, g = lane >> 4, q = (lane >> 2) & 3, pp = lane & 3;
  const rsrc_t rs = make_rsrc(p.ws, WS_END);
  const unsigned Lb = (unsigned)(size_t)L0;
  hold_back(rtc(), TAIL ? T_HOLD_TAIL : T_HOLD);
  sfor<0, 8>([&](auto I) { stage_chunk(p.wo + (size_t)(h * 128 + 64 * (I >> 2)) * 512 + 128 * (I & 3), 1024, Lb + I * 16384, w, lane); });
  WAIT_VM(0);
  BAR();
  v4u wf[4][2][4];
#pragma unroll
  for (int cg = 0; cg < 4; ++cg)
#pragma unroll
    for (int t = 0; t < 2; ++t)
#pragma unroll
      for (int kk = 0; kk < 4; ++kk) {
        const lptr slot = L0 + ((kk >> 1) * 4 + w) * 16384;
        const v2u a = trfrag(slot, 32 * (kk & 1) + 4 * g + q, 8 * cg + 2 * pp + t), b = trfrag(slot, 32 * (kk & 1) + 16 + 4 * g + q, 8 * cg + 2 * pp + t);
        wf[cg][t][kk] = v4u{a.x, a.y, b.x, b.y};
      }
  BAR();
  const lptr L = L0;
  const int T = w & 1, eh = w >> 1;
  const int qkvflag = OFF_FLAG + (FL_QKV + (lane % 3) * 32 + h) * 128;
  const int kmflag = OFF_KMF + 8192 * near_half(p, 0, lane) + (2 * h + (lane & 1)) * 128;
  const float cs = 0.08838834764831845f * 1.4426950408889634f;
  v4u t0[6], km; v2u vm;
  unsigned seenQ = 0, seenM = 0, flv = 0;
  bool pending = false;
  bool havekm = false;
  LAS unsigned* avail = (LAS unsigned*)(L + FA_AV);
  auto fetch_km = [&](int s) {
    km = ld16(rs, OFF_KM + (h * 64 + s) * 4096 + w * 1024 + lane * 16);
    vm = ld8(rs, OFF_VM + (h * 64 + s) * 2048 + w * 512 + lane * 8);
  };
  auto fetch = [&](int s) {
    if (seenQ < (unsigned)(s >> 1) + 1) seenQ = wait_flags(rs, qkvflag, (unsigned)(s >> 1) + 1, 0x400u + s);
    const int tb = qkv_block(s, h) + lane * 16;
#pragma unroll
    for (int k = 0; k < 2; ++k) {
      t0[k] = ld16(rs, OFF_Q + tb + (2 * w + k) * 1024);
      t0[2 + k] = ld16(rs, OFF_K + tb + (2 * w + k) * 1024);
      t0[4 + k] = ld16(rs, OFF_V + tb + (2 * w + k) * 1024);
    }
  };
  if (TAIL) {
    if (w == 0) wait_flags<32>(rs, kmflag, (unsigned)sbeg - 3, 0x580u);
    BAR();
  }
  fetch(sbeg);
  if (tid == 0) *avail = 0;
  int prev = 0;
#pragma unroll 1
  for (int s = sbeg; s < NSEG; s = next_seg<TAIL>(s)) {
    BAR();
    unsigned av = *avail;
    if (!havekm) {
      if (av < (unsigned)s + 1) {
        if (pending) {
          WAIT_VM(0);
          BAR();
          if (tid == 0) st4((unsigned)prev + 1, rs, OFF_FLAG + (fl + h) * 128);
          pending = false;
        }
        if (w == 0) { seenM = wait_flags(rs, kmflag, (unsigned)s + 1, 0x500u + s); if (lane == 0) *avail = seenM; }
        BAR();
        av = *avail;
      }
      fetch_km(s);
    }
#pragma unroll
    for (int k = 0; k < 2; ++k) {
      lds16w(L + FA_Q + (2 * w + k) * 1024 + lane * 16, t0[k]);
      lds16w(L + FA_K + (2 * w + k) * 1024 + lane * 16, t0[2 + k]);
      lds16w(L + FA_V + (2 * w + k) * 1024 + lane * 16, t0[4 + k]);
    }
    lds16w(L + FA_KM + w * 1024 + lane * 16, km);
    *(LAS v2u*)(L + FA_VM + w * 512 + lane * 8) = vm;
    BAR();
    const int nx = next_seg<TAIL>(s);
    havekm = nx < NSEG && av >= (unsigned)nx + 1;
    if (nx < NSEG) {
      fetch(nx);
      if (havekm) fetch_km(nx);
      if (w == 0) flv = ld4(rs, kmflag);
    }
    v4u qf[4], ka[4], kb[4], kc[4];
#pragma unroll
    for (int kk = 0; kk < 4; ++kk) {
      qf[kk] = lds16(L + FA_Q + (T * 4 + kk) * 1024 + lane * 16);
      ka[kk] = lds16(L + FA_K + kk * 1024 + lane * 16);
      kb[kk] = lds16(L + FA_K + (4 + kk) * 1024 + lane * 16);
      kc[kk] = lds16(L + FA_KM + kk * 1024 + lane * 16);
    }
    SB();
    f32x4 s0 = {0.f, 0.f, 0.f, 0.f}, s1 = s0, s2 = s0;
#pragma unroll
    for (int kk = 0; kk < 4; ++kk) {
      s0 = mfma32(ka[kk], qf[kk], s0);
      s1 = mfma32(kb[kk], qf[kk], s1);
      s2 = mfma32(kc[kk], qf[kk], s2);
    }
    SB();
    v4u va[4]; v2u zv[2];
#pragma unroll
    for (int k = 0; k < 4; ++k) va[k] = lds16(L + FA_V + (4 * eh + k) * 1024 + lane * 16);
#pragma unroll
    for (int k = 0; k < 2; ++k) zv[k] = *(LAS v2u*)(L + FA_VM + (2 * eh + k) * 512 + lane * 8);
    SB();
    const int tok = 16 * T + n;
    const float ninf = -__builtin_inff();
    float mx = fmaxf(s2[0], s2[1]);
#pragma unroll
    for (int r = 0; r < 4; ++r) {
      s0[r] = (4 * g + r <= tok) ? s0[r] : ninf;
      s1[r] = (16 + 4 * g + r <= tok) ? s1[r] : ninf;
      mx = fmaxf(mx, fmaxf(s0[r], s1[r]));
    }
    mx = max32(max16(mx));
    const float mc = mx * cs;
    float p0[4], p1[4];
#pragma unroll
    for (int r = 0; r < 4; ++r) {
      p0[r] = __builtin_amdgcn_exp2f(s0[r] * cs - mc);
      p1[r] = __builtin_amdgcn_exp2f(s1[r] * cs - mc);
    }
    const float p20 = __builtin_amdgcn_exp2f(s2[0] * cs - mc), p21 = __builtin_amdgcn_exp2f(s2[1] * cs - mc);
    float l = ((p0[0] + p0[1]) + (p0[2] + p0[3])) + ((p1[0] + p1[1]) + (p1[2] + p1[3])) + (p20 + p21);
    l = sum32(sum16(l));
    const float rl = __builtin_amdgcn_rcpf(l);
    const v4u pxf = {pk2(p0[0], p0[1]), pk2(p0[2], p0[3]), pk2(p1[0], p1[1]), pk2(p1[2], p1[3])};
    const unsigned m01 = pk2(p20, p21);
    const v2u pm0 = {m01, 0u}, pm1 = {0u, m01};
#pragma unroll
    for (int k = 0; k < 2; ++k) {
      const int kk = 2 * eh + k;
      const f32x4 z4 = {0.f, 0.f, 0.f, 0.f};
      f32x4 o0 = mfma32(va[2 * k], pxf, z4);
      f32x4 o1 = mfma32(va[2 * k + 1], pxf, z4);
      o0 = mfma16(zv[k], pm0, o0);
      o1 = mfma16(zv[k], pm1, o1);
      lds16w(L + FA_O + (T * 4 + kk) * 1024 + lane * 16,
             v4u{pk2(o0[0] * rl, o0[1] * rl), pk2(o0[2] * rl, o0[3] * rl), pk2(o1[0] * rl, o1[1] * rl), pk2(o1[2] * rl, o1[3] * rl)});
    }
    if (pending) { if (nx < NSEG) WAIT_VM(6); else WAIT_VM(0); }
    BAR();
    if (pending && tid == 0) st4((unsigned)prev + 1, rs, OFF_FLAG + (fl + h) * 128);
    pending = false;
    const int pbase = OFF_PART + (s * NPART + h) * 32768 + lane * 16;
    v4u of[2][4];
#pragma unroll
    for (int TT = 0; TT < 2; ++TT)
#pragma unroll
      for (int kk = 0; kk < 4; ++kk) of[TT][kk] = lds16(L + FA_O + (TT * 4 + kk) * 1024 + lane * 16);
    SB();
#pragma unroll
    for (int TT = 0; TT < 2; ++TT) {
#pragma unroll
      for (int cg = 0; cg < 4; ++cg) {
        f32x4 a = {0.f, 0.f, 0.f, 0.f}, b = a;
#pragma unroll
        for (int kk = 0; kk < 4; ++kk) { a = mfma32(wf[cg][0][kk], of[TT][kk], a); b = mfma32(wf[cg][1][kk], of[TT][kk], b); }
        st16(v4u{pk2(a[0], a[1]), pk2(a[2], a[3]), pk2(b[0], b[1]), pk2(b[2], b[3])}, rs, pbase + ((4 * w + cg) * 2 + TT) * 1024);
#define F_SPREAD 0
        if (F_SPREAD && s + 2 < NSEG) { SB(); __builtin_amdgcn_s_sleep(F_SPREAD); SB(); }
      }
    }
    if (w == 0 && nx < NSEG) { const unsigned m = wave_min(flv); seenM = m > seenM ? m : seenM; if (lane == 0) *avail = seenM; }
    pending = true;
    prev = s;
    WSTAMP(2 + s);
  }
  WAIT_VM(0);
  BAR();
  if (tid == 0) st4((unsigned)prev + 1, rs, OFF_FLAG + (fl + h) * 128);
}

template <bool TAIL> DEV void role_red(const Params& p, int u, lptr L, int fl) {
  constexpr int sbeg = TAIL ? NSEG - 3 : 0;
  const int tid = threadIdx.x, lane = tid & 63, w = __builtin_amdgcn_readfirstlane(tid >> 6);
  const int n = lane & 15, g = lane >> 4, cg = u >> 1, T = u & 1;
  const rsrc_t rs = make_rsrc(p.ws, WS_END);
  v4u slo, shi;
#pragma unroll
  for (int j = 0; j < 4; ++j) {
    const int k = 8 * g + 2 * j;
    slo[j] = (k == n ? 0x3F80u : 0u) | (k + 1 == n ? 0x3F800000u : 0u);
    shi[j] = (k == 16 + n ? 0x3F80u : 0u) | (k + 1 == 16 + n ? 0x3F800000u : 0u);
  }
  const int ftok = tid >> 4, fcp = tid & 15;
  const unsigned bv = *(const GAS unsigned*)((const GAS unsigned short*)p.b2 + 32 * cg + 2 * fcp);
  const int fflag = OFF_FLAG + (fl + (lane & 31)) * 128;
  if (TAIL) {
    if (w == 0) wait_flags<32>(rs, OFF_FLAG + (FL_F + (lane & 31)) * 128, (unsigned)sbeg - 3, 0x780u);
    BAR();
  }
#pragma unroll 1
  for (int s = sbeg; s < NSEG; s = next_seg<TAIL>(s)) {
    const size_t xo = (size_t)(32 * s + 16 * T + ftok) * 512 + 32 * cg + 2 * fcp;
    const unsigned xv = *(const GAS unsigned*)((const GAS unsigned short*)p.x + xo);
    const int pb = OFF_PART + s * NPART * 32768 + u * 1024 + lane * 16;
    if ((s & 1) == 0 || TAIL) wait_flags(rs, OFF_FLAG + (FL_MLP + (lane % 14)) * 128, (unsigned)(s >> 1) + 1, 0x600u + s);
    v4u em[4], e[8];
#pragma unroll
    for (int k = 0; k < 4; ++k) { const int pi = 32 + w + 4 * k; em[k] = ld16(rs, pb + (pi < NPART ? pi : 32) * 32768); }
    if (w == 0) wait_flags(rs, fflag, (unsigned)s + 1, 0x700u + s);
    BAR();
#pragma unroll
    for (int k = 0; k < 8; ++k) e[k] = ld16(rs, pb + (8 * w + k) * 32768);
    SB();
    f32x4 dlo = {0.f, 0.f, 0.f, 0.f}, dhi = dlo;
#pragma unroll
    for (int k = 0; k < 4; ++k) {
      if (32 + w + 4 * k < NPART) { dlo = mfma32(em[k], slo, dlo); dhi = mfma32(em[k], shi, dhi); }
    }
#pragma unroll
    for (int k = 0; k < 8; ++k) { dlo = mfma32(e[k], slo, dlo); dhi = mfma32(e[k], shi, dhi); }
    *(LAS f32x4*)(L + w * 2048 + n * 64 + g * 16) = dlo;
    *(LAS f32x4*)(L + w * 2048 + (16 + n) * 64 + g * 16) = dhi;
    BAR();
    float a = lo16(xv) + lo16(bv), b = hi16(xv) + hi16(bv);
#pragma unroll
    for (int ww = 0; ww < 4; ++ww) {
      a += *(LAS float*)(L + ww * 2048 + (2 * fcp) * 64 + ftok * 4);
      b += *(LAS float*)(L + ww * 2048 + (2 * fcp + 1) * 64 + ftok * 4);
    }
    *(GAS unsigned*)((GAS unsigned short*)p.out + xo) = pk2(a, b);
    WSTAMP(2 + s);
  }
}

__global__ void __launch_bounds__(256, 1) k_main(Params p) {
  extern __shared__ __attribute__((aligned(16))) unsigned char smem[];
  const lptr L = (lptr)smem;
  const int b = blockIdx.x, lo = b & 7, id = (b >> 3) * 4 + (lo & 3);
  WSTAMP(0);
  if (lo < 4) {
#define ABL 15
    if (id < 64) role_chain(p, 2 * ((id & 3) + 4 * (id >> 3)) + ((id >> 2) & 1), L);
    else if (id < 96) { if (ABL & 1) role_fatt<true>(p, id - 64, L, FL_FT); }
    else { if (ABL & 2) role_red<true>(p, id - 96, L, FL_FT); }
  } else {
    if (id < 32) role_qkv_t<true>(p, id, L);
    else if (id < 48) { if (ABL & 4) role_qkv_t<false>(p, id, L); }
    else if (id < 62) { if (ABL & 8) role_mlp(p, id - 48, L); }
    else if (id < 94) { if (ABL & 1) role_fatt<false>(p, id - 62, L, FL_F); }
    else if (id < 126) { if (ABL & 2) role_red<false>(p, id - 94, L, FL_F); }
  }
}

}

extern "C" void kernel_launch(void* const* d_in, const int* in_sizes, int n_in, void* d_out, int out_size, void* d_ws, size_t ws_size,
                              hipStream_t stream) {
  Params p;
  p.x = (const unsigned short*)d_in[0];
  p.mem0 = (const unsigned short*)d_in[1];
  p.wq = (const unsigned short*)d_in[2];
  p.wk = (const unsigned short*)d_in[3];
  p.wv = (const unsigned short*)d_in[4];
  p.wo = (const unsigned short*)d_in[5];
  p.w1 = (const unsigned short*)d_in[8];
  p.b1 = (const unsigned short*)d_in[9];
  p.w2 = (const unsigned short*)d_in[10];
  p.b2 = (const unsigned short*)d_in[11];
  p.out = (unsigned short*)d_out;
  const uintptr_t a = ((uintptr_t)d_ws + (2u << 20) - 1) & ~(uintptr_t)((2u << 20) - 1);
  p.ws = (unsigned char*)a;
  p.d1 = T_D1; p.d2 = T_D2; p.fb = T_DX;
  p.unused = 0;
  hipFuncSetAttribute((const void*)k_main, hipFuncAttributeMaxDynamicSharedMemorySize, LDS_BYTES);
  k_prep<<<256, 256, 0, stream>>>(p);
  k_main<<<256, 256, LDS_BYTES, stream>>>(p);
}
```
